```python
import torch

def _softmax(x, axis=-1):
    return torch.softmax(x, dim=axis)
def _gelu(x):
    return torch.nn.functional.gelu(x, approximate="tanh")
def _silu(x):
    return torch.nn.functional.silu(x)
def _sigmoid(x):
    return torch.sigmoid(x)
def _layernorm(x, axis=-1, eps=1e-5):
    m = x.mean(dim=axis, keepdim=True)
    v = x.var(dim=axis, keepdim=True, unbiased=False)
    return (x - m) / torch.sqrt(v + eps)
def _scatter_add(tgt, idx, src, axis=0):
    return tgt.clone().index_add_(axis, idx, src)
def _segment_sum(src, seg, n):
    out = torch.zeros((n,) + src.shape[1:], dtype=src.dtype)
    return out.index_add_(0, seg, src)
def _pad(x, pw):
    flat = tuple(v for pair in reversed(pw) for v in pair)
    return torch.nn.functional.pad(x, flat)

kernel_name = 'sequential_preLN__compressed_kv_cla__dense_activation_silu__decode'

def setup_inputs():

    _w = (lambda a: (lambda t: t.to(torch.bfloat16) if t.is_floating_point() else t)(torch.as_tensor(a)))
    def _A_setup():
        B, H, Sq, Sk, D, M = 4, 32, 1, 4096, 128, 640
        Dc = 4 * D
        return {
            "x": _w((lambda *s: torch.randn(*s, dtype=torch.bfloat16) * 0.05)(B, Sq, M)),
            "c_kv": _w((lambda *s: torch.randn(*s, dtype=torch.bfloat16) * 0.8)(B, Sk, Dc)),
            "w_q": _w((lambda *s: torch.randn(*s, dtype=torch.bfloat16) * 3.2)(M, H, D) * (M ** -0.5)),
            "w_uk": _w((lambda *s: torch.randn(*s, dtype=torch.bfloat16) * 3.2)(Dc, H, D) * (Dc ** -0.5)),
            "w_uv": _w((lambda *s: torch.randn(*s, dtype=torch.bfloat16) * 0.8)(Dc, H, D) * (Dc ** -0.5)),
            "w_o": _w((lambda *s: torch.randn(*s, dtype=torch.bfloat16) * 0.8)(H, D, M) * ((H * D) ** -0.5)),
        }
    _A_in = _A_setup()

    def _B_setup():
        B, S, D, F = 4, 1, 640, 1728
        return {
            "x": _w((lambda *s: torch.randn(*s, dtype=torch.bfloat16) * 0.05)(B, S, D)),
            "w1": _w((lambda *s: torch.randn(*s, dtype=torch.bfloat16) * 3.2)(D, F) * (D ** -0.5)),
            "w2": _w((lambda *s: torch.randn(*s, dtype=torch.bfloat16) * 0.8)(F, D) * (F ** -0.5)),
        }
    _B_in = _B_setup()

    return {
        "A_x": _A_in["x"],
        "A_c_kv": _A_in["c_kv"],
        "A_w_q": _A_in["w_q"],
        "A_w_uk": _A_in["w_uk"],
        "A_w_uv": _A_in["w_uv"],
        "A_w_o": _A_in["w_o"],
        "B_x": _B_in["x"],
        "B_w1": _B_in["w1"],
        "B_w2": _B_in["w2"],
    }

def reference(A_x, A_c_kv, A_w_q, A_w_uk, A_w_uv, A_w_o, B_x, B_w1, B_w2):
    def _A_ref(x, c_kv, w_q, w_uk, w_uv, w_o):
        B, Sq, M = x.shape[0], x.shape[1], x.shape[2]
        Sk = c_kv.shape[1]
        H, D = w_q.shape[1], w_q.shape[2]
        q = torch.einsum("bqm,mhd->bhqd", x, w_q)
        k = torch.einsum("bkc,chd->bhkd", c_kv, w_uk)
        v = torch.einsum("bkc,chd->bhkd", c_kv, w_uv)
        scores = torch.einsum("bhqd,bhkd->bhqk", q, k) * (D ** -0.5)
        off = Sk - Sq
        qi = torch.arange(Sq)[:, None]
        kj = torch.arange(Sk)[None, :]
        cmask = kj <= (qi + off)
        scores = torch.where(cmask[None, None], scores, -1e9)
        p = _softmax(scores, axis=-1)
        ctx = torch.einsum("bhqk,bhkd->bhqd", p, v)
        return torch.einsum("bhqd,hdm->bqm", ctx, w_o)

    def _B_ref(x, w1, w2):
        h = torch.einsum("bsd,df->bsf", x, w1)
        h = _silu(h)
        out = torch.einsum("bsf,fd->bsd", h, w2)
        return out

    x = A_x
    h = x + _A_ref(_layernorm(x, axis=-1), A_c_kv, A_w_q, A_w_uk, A_w_uv, A_w_o)
    h2 = h + _B_ref(_layernorm(h, axis=-1), B_w1, B_w2)
    return h2
```

```cpp
#include <hip/hip_runtime.h>
#include <hip/hip_bf16.h>
#include <stdint.h>

typedef unsigned short u16;
__device__ __forceinline__ float bf2f(u16 b) { return __uint_as_float(((unsigned)b) << 16); }
__device__ __forceinline__ u16 f2bf(float f) { unsigned u = __float_as_uint(f); u += 0x7fffu + ((u >> 16) & 1u); return (u16)(u >> 16); }
__device__ __forceinline__ float rbf(float f) { return bf2f(f2bf(f)); }

constexpr int B = 4, H = 32, SK = 4096, D = 128, M = 640, DC = 512, F = 1728;

__device__ void layernorm_row(const float* xin, float* xout, float* red) {
  int t = threadIdx.x;
  float s = 0.f;
  for (int i = t; i < M; i += 256) s += xin[i];
  red[t] = s; __syncthreads();
  for (int o = 128; o > 0; o >>= 1) { if (t < o) red[t] += red[t + o]; __syncthreads(); }
  float mean = red[0] / M; __syncthreads();
  float v = 0.f;
  for (int i = t; i < M; i += 256) { float d = xin[i] - mean; v += d * d; }
  red[t] = v; __syncthreads();
  for (int o = 128; o > 0; o >>= 1) { if (t < o) red[t] += red[t + o]; __syncthreads(); }
  float var = red[0] / M; __syncthreads();
  float mb = rbf(mean), vb = rbf(var);
  float den = rbf(sqrtf(rbf(vb + 1e-5f)));
  for (int i = t; i < M; i += 256) xout[i] = rbf(rbf(xin[i] - mb) / den);
  __syncthreads();
}

__global__ void k1(const u16* x, const u16* wq, const u16* wuk, float* qa) {
  __shared__ float xs[M], xn[M], red[256], q[D];
  int h = blockIdx.x, b = blockIdx.y, t = threadIdx.x;
  for (int i = t; i < M; i += 256) xs[i] = bf2f(x[b * M + i]);
  __syncthreads();
  layernorm_row(xs, xn, red);
  if (t < D) {
    float a = 0.f;
    for (int m = 0; m < M; ++m) a += xn[m] * bf2f(wq[(size_t)m * H * D + h * D + t]);
    q[t] = rbf(a);
  }
  __syncthreads();
  for (int c = t; c < DC; c += 256) {
    float a = 0.f;
    for (int d = 0; d < D; ++d) a += q[d] * bf2f(wuk[(size_t)c * H * D + h * D + d]);
    qa[((size_t)b * H + h) * DC + c] = a * 0.08838834764831845f;
  }
}
__global__ void k2(const float* qa, const u16* ckv, float* s) {
  int b = blockIdx.y, k0 = blockIdx.x * 64, t = threadIdx.x;
  int k = k0 + (t & 63);
  for (int h = t >> 6; h < H; h += 4) {
    float a = 0.f;
    for (int c = 0; c < DC; ++c) a += qa[((size_t)b * H + h) * DC + c] * bf2f(ckv[((size_t)b * SK + k) * DC + c]);
    s[((size_t)b * H + h) * SK + k] = a;
  }
}
__global__ void k3(const float* s, const u16* ckv, const u16* wuv, float* ctx) {
  __shared__ float p[SK], red[256], cc[DC];
  int h = blockIdx.x, b = blockIdx.y, t = threadIdx.x;
  const float* sr = s + ((size_t)b * H + h) * SK;
  float mx = -INFINITY;
  for (int k = t; k < SK; k += 256) mx = fmaxf(mx, sr[k]);
  red[t] = mx; __syncthreads();
  for (int o = 128; o > 0; o >>= 1) { if (t < o) red[t] = fmaxf(red[t], red[t + o]); __syncthreads(); }
  mx = red[0]; __syncthreads();
  float l = 0.f;
  for (int k = t; k < SK; k += 256) { float e = __expf(sr[k] - mx); p[k] = e; l += e; }
  red[t] = l; __syncthreads();
  for (int o = 128; o > 0; o >>= 1) { if (t < o) red[t] += red[t + o]; __syncthreads(); }
  l = red[0]; __syncthreads();
  for (int c = t; c < DC; c += 256) {
    float a = 0.f;
    for (int k = 0; k < SK; ++k) a += p[k] * bf2f(ckv[((size_t)b * SK + k) * DC + c]);
    cc[c] = a / l;
  }
  __syncthreads();
  if (t < D) {
    float a = 0.f;
    for (int c = 0; c < DC; ++c) a += cc[c] * bf2f(wuv[(size_t)c * H * D + h * D + t]);
    ctx[((size_t)b * H + h) * D + t] = rbf(a);
  }
}
__global__ void k4(const float* ctx, const u16* wo, const u16* x, float* hbuf) {
  __shared__ float red[256];
  int b = blockIdx.y, m = blockIdx.x * 64 + (threadIdx.x & 63), g = threadIdx.x >> 6;
  float a = 0.f;
  for (int i = g; i < H * D; i += 4) a += ctx[(size_t)b * H * D + i] * bf2f(wo[(size_t)i * M + m]);
  red[threadIdx.x] = a; __syncthreads();
  if (g == 0) {
    a = red[threadIdx.x] + red[threadIdx.x + 64] + red[threadIdx.x + 128] + red[threadIdx.x + 192];
    hbuf[b * M + m] = rbf(bf2f(x[b * M + m]) + rbf(a));
  }
}
__global__ void k5(const float* hbuf, const u16* w1, float* f) {
  __shared__ float xs[M], xn[M], red[256];
  int b = blockIdx.y, t = threadIdx.x;
  for (int i = t; i < M; i += 256) xs[i] = hbuf[b * M + i];
  __syncthreads();
  layernorm_row(xs, xn, red);
  int j = blockIdx.x * 64 + (t & 63), g = t >> 6;
  float a = 0.f;
  for (int m = g; m < M; m += 4) a += xn[m] * bf2f(w1[(size_t)m * F + j]);
  red[t] = a; __syncthreads();
  if (g == 0) {
    a = rbf(red[t] + red[t + 64] + red[t + 128] + red[t + 192]);
    f[b * F + j] = rbf(a / (1.f + expf(-a)));
  }
}
__global__ void k6(const float* f, const u16* w2, const float* hbuf, u16* out) {
  __shared__ float red[256];
  int b = blockIdx.y, t = threadIdx.x, m = blockIdx.x * 64 + (t & 63), g = t >> 6;
  float a = 0.f;
  for (int j = g; j < F; j += 4) a += f[b * F + j] * bf2f(w2[(size_t)j * M + m]);
  red[t] = a; __syncthreads();
  if (g == 0) {
    a = rbf(red[t] + red[t + 64] + red[t + 128] + red[t + 192]);
    out[b * M + m] = f2bf(hbuf[b * M + m] + a);
  }
}

extern "C" void kernel_launch(void* const* d_in, const int* in_sizes, int n_in,
                              void* d_out, int out_size, void* d_ws, size_t ws_size,
                              hipStream_t stream) {
  const u16* x = (const u16*)d_in[0];
  const u16* ckv = (const u16*)d_in[1];
  const u16* wq = (const u16*)d_in[2];
  const u16* wuk = (const u16*)d_in[3];
  const u16* wuv = (const u16*)d_in[4];
  const u16* wo = (const u16*)d_in[5];
  const u16* w1 = (const u16*)d_in[7];
  const u16* w2 = (const u16*)d_in[8];
  float* ws = (float*)d_ws;
  float* qa = ws;
  float* s = qa + B * H * DC;
  float* ctx = s + B * H * SK;
  float* hb = ctx + B * H * D;
  float* f = hb + B * M;
  k1<<<dim3(H, B), 256, 0, stream>>>(x, wq, wuk, qa);
  k2<<<dim3(SK / 64, B), 256, 0, stream>>>(qa, ckv, s);
  k3<<<dim3(H, B), 256, 0, stream>>>(s, ckv, wuv, ctx);
  k4<<<dim3(M / 64, B), 256, 0, stream>>>(ctx, wo, x, hb);
  k5<<<dim3(F / 64, B), 256, 0, stream>>>(hb, w1, f);
  k6<<<dim3(M / 64, B), 256, 0, stream>>>(f, w2, hb, (u16*)d_out);
}
```

```cpp
#include <hip/hip_runtime.h>
#include <stdint.h>

#define STR_(x) #x
#define STR(x) STR_(x)
#define T2_SLEEP 1
#define HINT_SLEEP 4

namespace mla {
typedef unsigned short u16;
typedef unsigned v4u __attribute__((ext_vector_type(4)));
typedef unsigned v2u __attribute__((ext_vector_type(2)));
typedef unsigned v32u __attribute__((ext_vector_type(32)));
typedef unsigned v16u __attribute__((ext_vector_type(16)));
typedef float f32x4 __attribute__((ext_vector_type(4)));
typedef float f32x2 __attribute__((ext_vector_type(2)));
typedef __bf16 bf16x8 __attribute__((ext_vector_type(8)));
typedef __bf16 bf16x2 __attribute__((ext_vector_type(2)));
typedef short s16x4 __attribute__((ext_vector_type(4)));
typedef __amdgpu_buffer_rsrc_t rsrc_t;
#define LAS __attribute__((address_space(3)))
#define DI __device__ __forceinline__

constexpr int NB = 4, NH = 32, SK = 4096, HD = 128, MD = 640, DC = 512, FD = 1728;
constexpr unsigned WS_EPOCH = 0;
constexpr unsigned WS_QX = 4096;
constexpr unsigned WS_EV = WS_QX + 32 * 8192;
constexpr unsigned WS_H1 = WS_EV + 2048;
constexpr unsigned WS_QA = WS_H1 + 8 * 128 * 128;
constexpr unsigned WS_FL = WS_QA + 4 * 32 * 1024;
constexpr unsigned WS_CX = WS_FL + 128 * 4 * 512;
constexpr unsigned WS_HP = WS_CX + 32 * 16 * 256;
constexpr unsigned WS_OQ = WS_HP + 4 * 8 * 2560;
constexpr unsigned WS_OP = 2u << 20;
constexpr unsigned WS_END = WS_OP + 128 * 32 * 1024;
static_assert(WS_OQ + 4 * 27 * 2560 <= WS_OP, "ws map");
#define FL_OFF(H, W, C) (WS_FL + (unsigned)((((C) * 4 + (W)) * 32 + (H)) * 16))
constexpr unsigned CK_STR = 1056;
constexpr unsigned L_CK = 0;
constexpr unsigned L_Q = 128 * CK_STR;
constexpr unsigned L_WQ = L_Q;
constexpr unsigned L_P = L_Q + 16 * 1040;
constexpr unsigned L_MX = L_P + 8320;
constexpr unsigned L_ST = L_MX + 1024;
constexpr unsigned L_CTL = L_ST + 64;
constexpr unsigned L_SINK = L_CTL + 192;
constexpr unsigned L_BYTES = L_SINK + 256;
constexpr unsigned L_P2 = 8448;
constexpr unsigned L_T3W = 17408;
constexpr unsigned L_RED3 = 99328, L_F = L_RED3 + 2304;
constexpr unsigned L_UK = 0;
constexpr unsigned L_XS = L_WQ + 20480;
constexpr unsigned L_QG = L_WQ;
constexpr unsigned L_T1W = 35328, L_T2W = 68096, L_RED2 = 0, L_HS = 133632, L_QO = L_QG + 16 * 272;
static_assert(L_BYTES <= 163840 && L_XS + 4 * 1296 <= L_ST, "lds map");
constexpr int C_XN = 0, C_QP = 1, C_QN = 2, C_OS = 3, C_WF = 4, C_CS = 8, C_CNT = 9, C_W1 = 10, C_EV = 11, C_XR = 12, C_HN = 13, C_CK = 16, C_CKA = 20, C_TF = 24;

constexpr unsigned CKV_AT = 170, QA_AT = 540, QT1_AT = 500, RES_AT = 750;
constexpr unsigned H1_D = 42, D_ATT = 230, D_CX = 190, D_HP = 325, D_OQ = 200;
constexpr int CKV_WIN = 6, T1_SLEEP = 2, KBIT = 4;
constexpr int AUX_NT = 2, AUX_SC1 = 16;
constexpr unsigned SPIN_CAP = 1u << 16;
constexpr unsigned BLANK = 0xffffffffu;

DI rsrc_t mk_rsrc(const void* p, unsigned bytes) { return __builtin_amdgcn_make_buffer_rsrc((void*)p, (short)0, (int)bytes, 0x00020000); }
DI v4u ld_sc1(rsrc_t r, unsigned off) { return __builtin_amdgcn_raw_buffer_load_b128(r, off, 0, AUX_SC1); }
DI void st_sc1(rsrc_t r, unsigned off, v4u v) { __builtin_amdgcn_raw_buffer_store_b128(v, r, off, 0, AUX_SC1); }
DI unsigned nb(unsigned x) { return x < BLANK ? x : BLANK - 1u; }
DI unsigned nbf(float x) { return nb(__float_as_uint(x)); }
DI void dma16(unsigned lds, unsigned voff, rsrc_t r, unsigned soff) {
  asm volatile("s_mov_b32 m0, %0\n\ts_nop 0\n\tbuffer_load_dwordx4 %1, %2, %3 offen nt lds" ::"s"(lds), "v"(voff), "s"(r), "s"(soff) : "memory");
}
template <int N, int LSTEP, int GSTEP, int WIN = 63> DI void dma_run(unsigned lds, unsigned voff, rsrc_t r, unsigned soff) {
  unsigned c;
  asm volatile("s_mov_b32 m0, %1\n\ts_mov_b32 %0, %c6\n\t1:\n\tbuffer_load_dwordx4 %3, %4, %2 offen nt lds\n\ts_add_u32 %2, %2, %c8\n\ts_add_u32 m0, m0, %c7\n\t"
               ".if %c5 < 32\n\ts_waitcnt vmcnt(%c5)\n\t.endif\n\ts_sub_u32 %0, %0, 1\n\ts_cbranch_scc0 1b"
               : "=&s"(c), "+s"(lds), "+s"(soff) : "v"(voff), "s"(r), "i"(WIN), "i"(N - 1), "i"(LSTEP), "i"(GSTEP) : "scc", "memory");
}
template <int N> DI void vm_wait() { asm volatile("s_waitcnt vmcnt(%0)" ::"i"(N) : "memory"); }
DI void lgkm0() { asm volatile("s_waitcnt lgkmcnt(0)" ::: "memory"); }
DI void bar() { asm volatile("s_waitcnt lgkmcnt(0)\n\ts_barrier" ::: "memory"); }
DI void fence_sched() { __builtin_amdgcn_sched_barrier(0); }
DI f32x4 mfma(v4u a, v4u b, f32x4 c) { return __builtin_amdgcn_mfma_f32_16x16x32_bf16(__builtin_bit_cast(bf16x8, a), __builtin_bit_cast(bf16x8, b), c, 0, 0, 0); }
DI unsigned pk(float lo, float hi) { f32x2 v = {lo, hi}; return __builtin_bit_cast(unsigned, __builtin_convertvector(v, bf16x2)); }
DI float lo16(unsigned u) { return __uint_as_float(u << 16); }
DI float hi16(unsigned u) { return __uint_as_float(u & 0xffff0000u); }
DI float sel(bool c, float a, float b) { return c ? a : b; }
DI float sum4(f32x4 a) { const f32x2 t = f32x2{a[0], a[1]} + f32x2{a[2], a[3]}; return t[0] + t[1]; }
DI float rcp(float x) { return __builtin_amdgcn_rcpf(x); }
DI float ex2(float x) { return __builtin_amdgcn_exp2f(x); }
DI float rbf(float x) { return lo16(pk(x, x)); }
DI v2u tr8(LAS char* p) { return __builtin_bit_cast(v2u, __builtin_amdgcn_ds_read_tr16_b64_v4i16((LAS s16x4*)p)); }
DI v4u tr16(LAS char* p, int second) { v2u a = tr8(p), b = tr8(p + second); return v4u{a.x, a.y, b.x, b.y}; }
DI float max2(float a, float b) { return __builtin_elementwise_maximum(a, b); }
DI float max3(float a, float b, float c) { return max2(max2(a, b), c); }
template <int CTRL> DI unsigned dppu(unsigned x) { return (unsigned)__builtin_amdgcn_update_dpp(0, (int)x, CTRL, 0xf, 0xf, true); }
template <int CTRL> DI float dpp(float x) { return __uint_as_float(dppu<CTRL>(__float_as_uint(x))); }
DI float row_sum(float x) { x += dpp<0x128>(x); x += dpp<0x124>(x); x += dpp<0x122>(x); x += dpp<0x121>(x); return x; }
DI float row_max8(float x) { x = max3(x, dpp<0x121>(x), dpp<0x122>(x)); x = max2(x, dpp<0x121>(x)); return max2(x, dpp<0x124>(x)); }
DI float row_sum8(float x) { x += dpp<0x124>(x); x += dpp<0x122>(x); x += dpp<0x121>(x); return x; }
DI unsigned add3(unsigned a, unsigned b, unsigned c) { return a + b + c; }
DI unsigned xor3(unsigned a, unsigned b, unsigned c) { return __builtin_amdgcn_bitop3_b32(a, b, c, 0x96); }
DI float x16_add(float a, float b) { auto r = __builtin_amdgcn_permlane16_swap(__float_as_uint(a), __float_as_uint(b), false, false); return __uint_as_float(r[0]) + __uint_as_float(r[1]); }
DI float x32_add(float a, float b) { auto r = __builtin_amdgcn_permlane32_swap(__float_as_uint(a), __float_as_uint(b), false, false); return __uint_as_float(r[0]) + __uint_as_float(r[1]); }
DI float x16_sum(float x) { return x16_add(x, x); }
DI float x32_sum(float x) { return x32_add(x, x); }
DI float x16_max(float x) { auto r = __builtin_amdgcn_permlane16_swap(__float_as_uint(x), __float_as_uint(x), false, false); return max2(__uint_as_float(r[0]), __uint_as_float(r[1])); }
DI float x32_max(float x) { auto r = __builtin_amdgcn_permlane32_swap(__float_as_uint(x), __float_as_uint(x), false, false); return max2(__uint_as_float(r[0]), __uint_as_float(r[1])); }
DI unsigned lds_ld(LAS unsigned* p) { return __hip_atomic_load(p, __ATOMIC_RELAXED, __HIP_MEMORY_SCOPE_WORKGROUP); }
DI void lds_st(LAS unsigned* p, unsigned v) { __hip_atomic_store(p, v, __ATOMIC_RELAXED, __HIP_MEMORY_SCOPE_WORKGROUP); }
DI unsigned lds_addr(LAS unsigned* p) { return (unsigned)(size_t)p; }
DI void lds_add1(LAS unsigned* p) {
  asm volatile("s_waitcnt lgkmcnt(0)\n\ts_mov_b64 exec, 1\n\tds_add_u32 %0, %1\n\ts_mov_b64 exec, -1" ::"v"(lds_addr(p)), "v"(1u) : "memory");
}
template <int SLEEP = 0> DI void lds_wait_ge(LAS unsigned* p, unsigned v) {
  unsigned t, s, c;
  asm volatile("s_mov_b32 %2, 0x100000\n\t1:\n\tds_read_b32 %0, %3\n\ts_waitcnt lgkmcnt(0)\n\tv_readfirstlane_b32 %1, %0\n\ts_cmp_ge_u32 %1, %4\n\ts_cbranch_scc1 2f\n\t"
               ".if %c5\n\ts_sleep %c5\n\t.endif\n\ts_sub_u32 %2, %2, 1\n\ts_cbranch_scc0 1b\n\t2:"
               : "=&v"(t), "=&s"(s), "=&s"(c) : "v"(lds_addr(p)), "s"(v), "i"(SLEEP) : "scc", "memory");
}
template <unsigned N> DI bool last_of(LAS unsigned* cnt) {
  unsigned t, s;
  asm volatile("s_waitcnt lgkmcnt(0)\n\ts_mov_b64 exec, 1\n\tds_add_rtn_u32 %0, %2, %3\n\ts_mov_b64 exec, -1\n\ts_waitcnt lgkmcnt(0)\n\tv_readfirstlane_b32 %1, %0"
               : "=&v"(t), "=s"(s) : "v"(lds_addr(cnt)), "v"(1u) : "memory");
  return s == N - 1u;
}
template <int STRIDE> DI void poll_tag1q(rsrc_t r, unsigned voff, unsigned tag, v4u (&v)[1]) {
  unsigned so, cnt, tv, tw; unsigned long long t64, any;
  asm volatile(
      "s_mov_b32 %[cnt], 0x10000\n\t"
      "buffer_load_dwordx4 v[252:255], %[vo], %[rs], 0 offen sc1\n\t"
      "1:\n\t"
      "s_waitcnt vmcnt(0)\n\t"
      "s_mov_b64 %[any], 0\n\t"
      "v_cmp_ne_u32 vcc, v253, %[tag]\n\t"
      "v_cmp_ne_u32 %[t64], v255, %[tag]\n\t"
      "s_or_b64 vcc, vcc, %[t64]\n\t"
      "s_cbranch_scc0 2f\n\t"
      "s_mov_b64 %[any], vcc\n\t"
      "s_mov_b64 exec, vcc\n\t"
      "buffer_load_dwordx4 v[252:255], %[vo], %[rs], 0 offen sc1\n\t"
      "s_mov_b64 exec, -1\n\t"
      "2:\n\t"
      "s_cmp_eq_u64 %[any], 0\n\t"
      "s_cbranch_scc1 3f\n\t"
      "s_sub_u32 %[cnt], %[cnt], 1\n\t"
      "s_cmp_lg_u32 %[cnt], 0\n\t"
      "s_cbranch_scc1 1b\n\t"
      "s_waitcnt vmcnt(0)\n\t"
      "3:\n\t"
      : "=&{v[252:255]}"(v[0]), [so] "=&s"(so), [cnt] "=&s"(cnt), [tv] "=&v"(tv), [tw] "=&v"(tw), [t64] "=&s"(t64), [any] "=&s"(any)
      : [vo] "v"(voff), [rs] "s"(r), [st] "i"(STRIDE), [tag] "v"(tag)
      : "vcc", "scc", "memory");
}
template <int STRIDE> DI void poll_tag1h(rsrc_t r, unsigned voff, unsigned tag, v4u (&v)[1]) {
  unsigned so, cnt, tv, tw; unsigned long long t64, any;
  asm volatile(
      "s_mov_b32 %[cnt], 0x10000\n\t"
      "buffer_load_dwordx4 v[252:255], %[vo], %[rs], 0 offen sc1\n\t"
      "1:\n\t"
      "s_waitcnt vmcnt(0)\n\t"
      "s_mov_b64 %[any], 0\n\t"
      "v_cmp_ne_u32 vcc, v253, %[tag]\n\t"
      "v_cmp_ne_u32 %[t64], v255, %[tag]\n\t"
      "s_or_b64 vcc, vcc, %[t64]\n\t"
      "s_cbranch_scc0 2f\n\t"
      "s_sleep " STR(HINT_SLEEP) "\n\t"
      "s_mov_b64 %[any], vcc\n\t"
      "s_mov_b64 exec, vcc\n\t"
      "buffer_load_dwordx4 v[252:255], %[vo], %[rs], 0 offen sc1\n\t"
      "s_mov_b64 exec, -1\n\t"
      "2:\n\t"
      "s_cmp_eq_u64 %[any], 0\n\t"
      "s_cbranch_scc1 3f\n\t"
      "s_sub_u32 %[cnt], %[cnt], 1\n\t"
      "s_cmp_lg_u32 %[cnt], 0\n\t"
      "s_cbranch_scc1 1b\n\t"
      "s_waitcnt vmcnt(0)\n\t"
      "3:\n\t"
      : "=&{v[252:255]}"(v[0]), [so] "=&s"(so), [cnt] "=&s"(cnt), [tv] "=&v"(tv), [tw] "=&v"(tw), [t64] "=&s"(t64), [any] "=&s"(any)
      : [vo] "v"(voff), [rs] "s"(r), [st] "i"(STRIDE), [tag] "v"(tag)
      : "vcc", "scc", "memory");
}
template <int STRIDE> DI void poll_tag2s(rsrc_t r, unsigned voff, unsigned tag, v4u (&v)[2]) {
  unsigned so, cnt, tv, tw; unsigned long long t64, any;
  asm volatile(
      "s_mov_b32 %[cnt], 0x10000\n\t"
      "buffer_load_dwordx4 v[248:251], %[vo], %[rs], 0 offen sc1\n\t"
      "s_mov_b32 %[so], %c[st]*1\n\tbuffer_load_dwordx4 v[252:255], %[vo], %[rs], %[so] offen sc1\n\t"
      "1:\n\t"
      "s_waitcnt vmcnt(0)\n\t"
      "s_mov_b64 %[any], 0\n\t"
      "v_cmp_ne_u32 vcc, v249, %[tag]\n\t"
      "v_cmp_ne_u32 %[t64], v251, %[tag]\n\t"
      "s_or_b64 vcc, vcc, %[t64]\n\t"
      "s_cbranch_scc0 2f\n\t"
      "s_mov_b64 %[any], vcc\n\t"
      "s_mov_b64 exec, vcc\n\t"
      "buffer_load_dwordx4 v[248:251], %[vo], %[rs], 0 offen sc1\n\t"
      "s_mov_b64 exec, -1\n\t"
      "2:\n\t"
      "v_cmp_ne_u32 vcc, v253, %[tag]\n\t"
      "v_cmp_ne_u32 %[t64], v255, %[tag]\n\t"
      "s_or_b64 vcc, vcc, %[t64]\n\t"
      "s_cbranch_scc0 3f\n\t"
      "s_mov_b64 %[any], vcc\n\t"
      "s_mov_b64 exec, vcc\n\t"
      "s_mov_b32 %[so], %c[st]*1\n\tbuffer_load_dwordx4 v[252:255], %[vo], %[rs], %[so] offen sc1\n\t"
      "s_mov_b64 exec, -1\n\t"
      "3:\n\t"
      "s_cmp_eq_u64 %[any], 0\n\t"
      "s_cbranch_scc1 4f\n\t"
      "s_sleep 8\n\t"
      "s_sub_u32 %[cnt], %[cnt], 1\n\t"
      "s_cmp_lg_u32 %[cnt], 0\n\t"
      "s_cbranch_scc1 1b\n\t"
      "s_waitcnt vmcnt(0)\n\t"
      "4:\n\t"
      : "=&{v[248:251]}"(v[0]), "=&{v[252:255]}"(v[1]), [so] "=&s"(so), [cnt] "=&s"(cnt), [tv] "=&v"(tv), [tw] "=&v"(tw), [t64] "=&s"(t64), [any] "=&s"(any)
      : [vo] "v"(voff), [rs] "s"(r), [st] "i"(STRIDE), [tag] "v"(tag)
      : "vcc", "scc", "memory");
}
template <int STRIDE> DI void poll_blank2s(rsrc_t r, unsigned voff, v4u (&v)[2]) {
  unsigned so, cnt, tv, tw; unsigned long long t64, any;
  asm volatile(
      "s_mov_b32 %[cnt], 0x10000\n\t"
      "buffer_load_dwordx4 v[248:251], %[vo], %[rs], 0 offen sc1\n\t"
      "s_mov_b32 %[so], %c[st]*1\n\tbuffer_load_dwordx4 v[252:255], %[vo], %[rs], %[so] offen sc1\n\t"
      "1:\n\t"
      "s_waitcnt vmcnt(0)\n\t"
      "v_max3_u32 %[tv], v248, v249, v250\n\t"
      "v_max3_u32 %[tw], v251, v252, v253\n\t"
      "v_max3_u32 %[tv], %[tv], v254, v255\n\t"
      "v_max_u32 %[tv], %[tv], %[tw]\n\t"
      "v_cmp_eq_u32 vcc, -1, %[tv]\n\t"
      "s_and_b64 vcc, vcc, vcc\n\t"
      "s_cbranch_scc0 4f\n\t"
      "s_mov_b64 %[any], 0\n\t"
      "v_max3_u32 %[tv], v248, v249, v250\n\t"
      "v_max_u32 %[tv], %[tv], v251\n\t"
      "v_cmp_eq_u32 vcc, -1, %[tv]\n\t"
      "s_and_b64 vcc, vcc, vcc\n\t"
      "s_cbranch_scc0 2f\n\t"
      "s_mov_b64 %[any], vcc\n\t"
      "s_mov_b64 exec, vcc\n\t"
      "buffer_load_dwordx4 v[248:251], %[vo], %[rs], 0 offen sc1\n\t"
      "s_mov_b64 exec, -1\n\t"
      "2:\n\t"
      "v_max3_u32 %[tv], v252, v253, v254\n\t"
      "v_max_u32 %[tv], %[tv], v255\n\t"
      "v_cmp_eq_u32 vcc, -1, %[tv]\n\t"
      "s_and_b64 vcc, vcc, vcc\n\t"
      "s_cbranch_scc0 3f\n\t"
      "s_mov_b64 %[any], vcc\n\t"
      "s_mov_b64 exec, vcc\n\t"
      "s_mov_b32 %[so], %c[st]*1\n\tbuffer_load_dwordx4 v[252:255], %[vo], %[rs], %[so] offen sc1\n\t"
      "s_mov_b64 exec, -1\n\t"
      "3:\n\t"
      "s_cmp_eq_u64 %[any], 0\n\t"
      "s_cbranch_scc1 4f\n\t"
      "s_sleep " STR(T2_SLEEP) "\n\t"
      "s_sub_u32 %[cnt], %[cnt], 1\n\t"
      "s_cmp_lg_u32 %[cnt], 0\n\t"
      "s_cbranch_scc1 1b\n\t"
      "s_waitcnt vmcnt(0)\n\t"
      "4:\n\t"
      : "=&{v[248:251]}"(v[0]), "=&{v[252:255]}"(v[1]), [so] "=&s"(so), [cnt] "=&s"(cnt), [tv] "=&v"(tv), [tw] "=&v"(tw), [t64] "=&s"(t64), [any] "=&s"(any)
      : [vo] "v"(voff), [rs] "s"(r), [st] "i"(STRIDE)
      : "vcc", "scc", "memory");
}
template <int STRIDE> DI void poll_blank4m(rsrc_t r, unsigned voff, unsigned long long last_mask, v4u (&v)[4]) {
  unsigned so, cnt, tv, tw; unsigned long long t64, any;
  asm volatile(
      "s_mov_b32 %[cnt], 0x10000\n\t"
      "buffer_load_dwordx4 v[240:243], %[vo], %[rs], 0 offen sc1\n\t"
      "s_mov_b32 %[so], %c[st]*1\n\tbuffer_load_dwordx4 v[244:247], %[vo], %[rs], %[so] offen sc1\n\t"
      "s_mov_b32 %[so], %c[st]*2\n\tbuffer_load_dwordx4 v[248:251], %[vo], %[rs], %[so] offen sc1\n\t"
      "v_mov_b32 v252, 0\n\t"
      "v_mov_b32 v253, 0\n\t"
      "v_mov_b32 v254, 0\n\t"
      "v_mov_b32 v255, 0\n\t"
      "s_mov_b64 exec, %[lm]\n\t"
      "s_mov_b32 %[so], %c[st]*3\n\tbuffer_load_dwordx4 v[252:255], %[vo], %[rs], %[so] offen sc1\n\t"
      "s_mov_b64 exec, -1\n\t"
      "1:\n\t"
      "s_waitcnt vmcnt(0)\n\t"
      "v_max3_u32 %[tv], v240, v241, v242\n\t"
      "v_max3_u32 %[tw], v243, v244, v245\n\t"
      "v_max3_u32 %[tv], %[tv], v246, v247\n\t"
      "v_max3_u32 %[tw], %[tw], v248, v249\n\t"
      "v_max3_u32 %[tv], %[tv], v250, v251\n\t"
      "v_max3_u32 %[tw], %[tw], v252, v253\n\t"
      "v_max3_u32 %[tv], %[tv], v254, v255\n\t"
      "v_max_u32 %[tv], %[tv], %[tw]\n\t"
      "v_cmp_eq_u32 vcc, -1, %[tv]\n\t"
      "s_and_b64 vcc, vcc, vcc\n\t"
      "s_cbranch_scc0 6f\n\t"
      "s_mov_b64 %[any], 0\n\t"
      "v_max3_u32 %[tv], v240, v241, v242\n\t"
      "v_max_u32 %[tv], %[tv], v243\n\t"
      "v_cmp_eq_u32 vcc, -1, %[tv]\n\t"
      "s_and_b64 vcc, vcc, vcc\n\t"
      "s_cbranch_scc0 2f\n\t"
      "s_mov_b64 %[any], vcc\n\t"
      "s_mov_b64 exec, vcc\n\t"
      "buffer_load_dwordx4 v[240:243], %[vo], %[rs], 0 offen sc1\n\t"
      "s_mov_b64 exec, -1\n\t"
      "2:\n\t"
      "v_max3_u32 %[tv], v244, v245, v246\n\t"
      "v_max_u32 %[tv], %[tv], v247\n\t"
      "v_cmp_eq_u32 vcc, -1, %[tv]\n\t"
      "s_and_b64 vcc, vcc, vcc\n\t"
      "s_cbranch_scc0 3f\n\t"
      "s_mov_b64 %[any], vcc\n\t"
      "s_mov_b64 exec, vcc\n\t"
      "s_mov_b32 %[so], %c[st]*1\n\tbuffer_load_dwordx4 v[244:247], %[vo], %[rs], %[so] offen sc1\n\t"
      "s_mov_b64 exec, -1\n\t"
      "3:\n\t"
      "v_max3_u32 %[tv], v248, v249, v250\n\t"
      "v_max_u32 %[tv], %[tv], v251\n\t"
      "v_cmp_eq_u32 vcc, -1, %[tv]\n\t"
      "s_and_b64 vcc, vcc, vcc\n\t"
      "s_cbranch_scc0 4f\n\t"
      "s_mov_b64 %[any], vcc\n\t"
      "s_mov_b64 exec, vcc\n\t"
      "s_mov_b32 %[so], %c[st]*2\n\tbuffer_load_dwordx4 v[248:251], %[vo], %[rs], %[so] offen sc1\n\t"
      "s_mov_b64 exec, -1\n\t"
      "4:\n\t"
      "v_max3_u32 %[tv], v252, v253, v254\n\t"
      "v_max_u32 %[tv], %[tv], v255\n\t"
      "v_cmp_eq_u32 vcc, -1, %[tv]\n\t"
      "s_and_b64 vcc, vcc, vcc\n\t"
      "s_cbranch_scc0 5f\n\t"
      "s_mov_b64 %[any], vcc\n\t"
      "s_mov_b64 exec, vcc\n\t"
      "s_mov_b32 %[so], %c[st]*3\n\tbuffer_load_dwordx4 v[252:255], %[vo], %[rs], %[so] offen sc1\n\t"
      "s_mov_b64 exec, -1\n\t"
      "5:\n\t"
      "s_cmp_eq_u64 %[any], 0\n\t"
      "s_cbranch_scc1 6f\n\t"
      "s_sleep 1\n\t"
      "s_sub_u32 %[cnt], %[cnt], 1\n\t"
      "s_cmp_lg_u32 %[cnt], 0\n\t"
      "s_cbranch_scc1 1b\n\t"
      "s_waitcnt vmcnt(0)\n\t"
      "6:\n\t"
      : "=&{v[240:243]}"(v[0]), "=&{v[244:247]}"(v[1]), "=&{v[248:251]}"(v[2]), "=&{v[252:255]}"(v[3]), [so] "=&s"(so), [cnt] "=&s"(cnt), [tv] "=&v"(tv), [tw] "=&v"(tw), [t64] "=&s"(t64), [any] "=&s"(any)
      : [vo] "v"(voff), [rs] "s"(r), [st] "i"(STRIDE), [lm] "s"(last_mask)
      : "vcc", "scc", "memory");
}
template <int STRIDE> DI void poll_blank1s(rsrc_t r, unsigned voff, v4u (&v)[1]) {
  unsigned so, cnt, tv, tw; unsigned long long t64, any;
  asm volatile(
      "s_mov_b32 %[cnt], 0x10000\n\t"
      "buffer_load_dwordx4 v[252:255], %[vo], %[rs], 0 offen sc1\n\t"
      "1:\n\t"
      "s_waitcnt vmcnt(0)\n\t"
      "s_mov_b64 %[any], 0\n\t"
      "v_max3_u32 %[tv], v252, v253, v254\n\t"
      "v_max_u32 %[tv], %[tv], v255\n\t"
      "v_cmp_eq_u32 vcc, -1, %[tv]\n\t"
      "s_and_b64 vcc, vcc, vcc\n\t"
      "s_cbranch_scc0 2f\n\t"
      "s_sleep 2\n\t"
      "s_mov_b64 %[any], vcc\n\t"
      "s_mov_b64 exec, vcc\n\t"
      "buffer_load_dwordx4 v[252:255], %[vo], %[rs], 0 offen sc1\n\t"
      "s_mov_b64 exec, -1\n\t"
      "2:\n\t"
      "s_cmp_eq_u64 %[any], 0\n\t"
      "s_cbranch_scc1 3f\n\t"
      "s_sub_u32 %[cnt], %[cnt], 1\n\t"
      "s_cmp_lg_u32 %[cnt], 0\n\t"
      "s_cbranch_scc1 1b\n\t"
      "s_waitcnt vmcnt(0)\n\t"
      "3:\n\t"
      : "=&{v[252:255]}"(v[0]), [so] "=&s"(so), [cnt] "=&s"(cnt), [tv] "=&v"(tv), [tw] "=&v"(tw), [t64] "=&s"(t64), [any] "=&s"(any)
      : [vo] "v"(voff), [rs] "s"(r), [st] "i"(STRIDE)
      : "vcc", "scc", "memory");
}
template <int STRIDE> DI void poll_blank8(rsrc_t r, unsigned voff, v4u (&v)[8]) {
  unsigned so, cnt, tv, tw; unsigned long long t64, any;
  asm volatile(
      "s_mov_b32 %[cnt], 0x10000\n\t"
      "buffer_load_dwordx4 v[224:227], %[vo], %[rs], 0 offen sc1\n\t"
      "s_mov_b32 %[so], %c[st]*1\n\tbuffer_load_dwordx4 v[228:231], %[vo], %[rs], %[so] offen sc1\n\t"
      "s_mov_b32 %[so], %c[st]*2\n\tbuffer_load_dwordx4 v[232:235], %[vo], %[rs], %[so] offen sc1\n\t"
      "s_mov_b32 %[so], %c[st]*3\n\tbuffer_load_dwordx4 v[236:239], %[vo], %[rs], %[so] offen sc1\n\t"
      "s_mov_b32 %[so], %c[st]*4\n\tbuffer_load_dwordx4 v[240:243], %[vo], %[rs], %[so] offen sc1\n\t"
      "s_mov_b32 %[so], %c[st]*5\n\tbuffer_load_dwordx4 v[244:247], %[vo], %[rs], %[so] offen sc1\n\t"
      "s_mov_b32 %[so], %c[st]*6\n\tbuffer_load_dwordx4 v[248:251], %[vo], %[rs], %[so] offen sc1\n\t"
      "s_mov_b32 %[so], %c[st]*7\n\tbuffer_load_dwordx4 v[252:255], %[vo], %[rs], %[so] offen sc1\n\t"
      "1:\n\t"
      "s_waitcnt vmcnt(0)\n\t"
      "v_max3_u32 %[tv], v224, v225, v226\n\t"
      "v_max3_u32 %[tw], v227, v228, v229\n\t"
      "v_max3_u32 %[tv], %[tv], v230, v231\n\t"
      "v_max3_u32 %[tw], %[tw], v232, v233\n\t"
      "v_max3_u32 %[tv], %[tv], v234, v235\n\t"
      "v_max3_u32 %[tw], %[tw], v236, v237\n\t"
      "v_max3_u32 %[tv], %[tv], v238, v239\n\t"
      "v_max3_u32 %[tw], %[tw], v240, v241\n\t"
      "v_max3_u32 %[tv], %[tv], v242, v243\n\t"
      "v_max3_u32 %[tw], %[tw], v244, v245\n\t"
      "v_max3_u32 %[tv], %[tv], v246, v247\n\t"
      "v_max3_u32 %[tw], %[tw], v248, v249\n\t"
      "v_max3_u32 %[tv], %[tv], v250, v251\n\t"
      "v_max3_u32 %[tw], %[tw], v252, v253\n\t"
      "v_max3_u32 %[tv], %[tv], v254, v255\n\t"
      "v_max_u32 %[tv], %[tv], %[tw]\n\t"
      "v_cmp_eq_u32 vcc, -1, %[tv]\n\t"
      "s_and_b64 vcc, vcc, vcc\n\t"
      "s_cbranch_scc0 10f\n\t"
      "s_mov_b64 %[any], 0\n\t"
      "v_max3_u32 %[tv], v224, v225, v226\n\t"
      "v_max_u32 %[tv], %[tv], v227\n\t"
      "v_cmp_eq_u32 vcc, -1, %[tv]\n\t"
      "s_and_b64 vcc, vcc, vcc\n\t"
      "s_cbranch_scc0 2f\n\t"
      "s_mov_b64 %[any], vcc\n\t"
      "s_mov_b64 exec, vcc\n\t"
      "buffer_load_dwordx4 v[224:227], %[vo], %[rs], 0 offen sc1\n\t"
      "s_mov_b64 exec, -1\n\t"
      "2:\n\t"
      "v_max3_u32 %[tv], v228, v229, v230\n\t"
      "v_max_u32 %[tv], %[tv], v231\n\t"
      "v_cmp_eq_u32 vcc, -1, %[tv]\n\t"
      "s_and_b64 vcc, vcc, vcc\n\t"
      "s_cbranch_scc0 3f\n\t"
      "s_mov_b64 %[any], vcc\n\t"
      "s_mov_b64 exec, vcc\n\t"
      "s_mov_b32 %[so], %c[st]*1\n\tbuffer_load_dwordx4 v[228:231], %[vo], %[rs], %[so] offen sc1\n\t"
      "s_mov_b64 exec, -1\n\t"
      "3:\n\t"
      "v_max3_u32 %[tv], v232, v233, v234\n\t"
      "v_max_u32 %[tv], %[tv], v235\n\t"
      "v_cmp_eq_u32 vcc, -1, %[tv]\n\t"
      "s_and_b64 vcc, vcc, vcc\n\t"
      "s_cbranch_scc0 4f\n\t"
      "s_mov_b64 %[any], vcc\n\t"
      "s_mov_b64 exec, vcc\n\t"
      "s_mov_b32 %[so], %c[st]*2\n\tbuffer_load_dwordx4 v[232:235], %[vo], %[rs], %[so] offen sc1\n\t"
      "s_mov_b64 exec, -1\n\t"
      "4:\n\t"
      "v_max3_u32 %[tv], v236, v237, v238\n\t"
      "v_max_u32 %[tv], %[tv], v239\n\t"
      "v_cmp_eq_u32 vcc, -1, %[tv]\n\t"
      "s_and_b64 vcc, vcc, vcc\n\t"
      "s_cbranch_scc0 5f\n\t"
      "s_mov_b64 %[any], vcc\n\t"
      "s_mov_b64 exec, vcc\n\t"
      "s_mov_b32 %[so], %c[st]*3\n\tbuffer_load_dwordx4 v[236:239], %[vo], %[rs], %[so] offen sc1\n\t"
      "s_mov_b64 exec, -1\n\t"
      "5:\n\t"
      "v_max3_u32 %[tv], v240, v241, v242\n\t"
      "v_max_u32 %[tv], %[tv], v243\n\t"
      "v_cmp_eq_u32 vcc, -1, %[tv]\n\t"
      "s_and_b64 vcc, vcc, vcc\n\t"
      "s_cbranch_scc0 6f\n\t"
      "s_mov_b64 %[any], vcc\n\t"
      "s_mov_b64 exec, vcc\n\t"
      "s_mov_b32 %[so], %c[st]*4\n\tbuffer_load_dwordx4 v[240:243], %[vo], %[rs], %[so] offen sc1\n\t"
      "s_mov_b64 exec, -1\n\t"
      "6:\n\t"
      "v_max3_u32 %[tv], v244, v245, v246\n\t"
      "v_max_u32 %[tv], %[tv], v247\n\t"
      "v_cmp_eq_u32 vcc, -1, %[tv]\n\t"
      "s_and_b64 vcc, vcc, vcc\n\t"
      "s_cbranch_scc0 7f\n\t"
      "s_mov_b64 %[any], vcc\n\t"
      "s_mov_b64 exec, vcc\n\t"
      "s_mov_b32 %[so], %c[st]*5\n\tbuffer_load_dwordx4 v[244:247], %[vo], %[rs], %[so] offen sc1\n\t"
      "s_mov_b64 exec, -1\n\t"
      "7:\n\t"
      "v_max3_u32 %[tv], v248, v249, v250\n\t"
      "v_max_u32 %[tv], %[tv], v251\n\t"
      "v_cmp_eq_u32 vcc, -1, %[tv]\n\t"
      "s_and_b64 vcc, vcc, vcc\n\t"
      "s_cbranch_scc0 8f\n\t"
      "s_mov_b64 %[any], vcc\n\t"
      "s_mov_b64 exec, vcc\n\t"
      "s_mov_b32 %[so], %c[st]*6\n\tbuffer_load_dwordx4 v[248:251], %[vo], %[rs], %[so] offen sc1\n\t"
      "s_mov_b64 exec, -1\n\t"
      "8:\n\t"
      "v_max3_u32 %[tv], v252, v253, v254\n\t"
      "v_max_u32 %[tv], %[tv], v255\n\t"
      "v_cmp_eq_u32 vcc, -1, %[tv]\n\t"
      "s_and_b64 vcc, vcc, vcc\n\t"
      "s_cbranch_scc0 9f\n\t"
      "s_mov_b64 %[any], vcc\n\t"
      "s_mov_b64 exec, vcc\n\t"
      "s_mov_b32 %[so], %c[st]*7\n\tbuffer_load_dwordx4 v[252:255], %[vo], %[rs], %[so] offen sc1\n\t"
      "s_mov_b64 exec, -1\n\t"
      "9:\n\t"
      "s_cmp_eq_u64 %[any], 0\n\t"
      "s_cbranch_scc1 10f\n\t"
      "s_sleep 1\n\t"
      "s_sub_u32 %[cnt], %[cnt], 1\n\t"
      "s_cmp_lg_u32 %[cnt], 0\n\t"
      "s_cbranch_scc1 1b\n\t"
      "s_waitcnt vmcnt(0)\n\t"
      "10:\n\t"
      : "=&{v[224:227]}"(v[0]), "=&{v[228:231]}"(v[1]), "=&{v[232:235]}"(v[2]), "=&{v[236:239]}"(v[3]), "=&{v[240:243]}"(v[4]), "=&{v[244:247]}"(v[5]), "=&{v[248:251]}"(v[6]), "=&{v[252:255]}"(v[7]), [so] "=&s"(so), [cnt] "=&s"(cnt), [tv] "=&v"(tv), [tw] "=&v"(tw), [t64] "=&s"(t64), [any] "=&s"(any)
      : [vo] "v"(voff), [rs] "s"(r), [st] "i"(STRIDE)
      : "vcc", "scc", "memory");
}
template <int STRIDE> DI void issue_blank8(rsrc_t r, unsigned voff, v4u (&v)[8]) {
  unsigned so, cnt, tv, tw; unsigned long long t64, any;
  asm volatile(
      "buffer_load_dwordx4 v[224:227], %[vo], %[rs], 0 offen sc1\n\t"
      "s_mov_b32 %[so], %c[st]*1\n\tbuffer_load_dwordx4 v[228:231], %[vo], %[rs], %[so] offen sc1\n\t"
      "s_mov_b32 %[so], %c[st]*2\n\tbuffer_load_dwordx4 v[232:235], %[vo], %[rs], %[so] offen sc1\n\t"
      "s_mov_b32 %[so], %c[st]*3\n\tbuffer_load_dwordx4 v[236:239], %[vo], %[rs], %[so] offen sc1\n\t"
      "s_mov_b32 %[so], %c[st]*4\n\tbuffer_load_dwordx4 v[240:243], %[vo], %[rs], %[so] offen sc1\n\t"
      "s_mov_b32 %[so], %c[st]*5\n\tbuffer_load_dwordx4 v[244:247], %[vo], %[rs], %[so] offen sc1\n\t"
      "s_mov_b32 %[so], %c[st]*6\n\tbuffer_load_dwordx4 v[248:251], %[vo], %[rs], %[so] offen sc1\n\t"
      "s_mov_b32 %[so], %c[st]*7\n\tbuffer_load_dwordx4 v[252:255], %[vo], %[rs], %[so] offen sc1\n\t"
      : "=&{v[224:227]}"(v[0]), "=&{v[228:231]}"(v[1]), "=&{v[232:235]}"(v[2]), "=&{v[236:239]}"(v[3]), "=&{v[240:243]}"(v[4]), "=&{v[244:247]}"(v[5]), "=&{v[248:251]}"(v[6]), "=&{v[252:255]}"(v[7]), [so] "=&s"(so)
      : [vo] "v"(voff), [rs] "s"(r), [st] "i"(STRIDE) : "memory");
  (void)cnt; (void)tv; (void)tw; (void)t64; (void)any;
}
template <int STRIDE> DI void finish_blank8(rsrc_t r, unsigned voff, v4u (&v)[8]) {
  unsigned so, cnt, tv, tw; unsigned long long t64, any;
  asm volatile(
      "s_mov_b32 %[cnt], 0x10000\n\t"
      "1:\n\t"
      "s_waitcnt vmcnt(0)\n\t"
      "v_max3_u32 %[tv], v224, v225, v226\n\t"
      "v_max3_u32 %[tw], v227, v228, v229\n\t"
      "v_max3_u32 %[tv], %[tv], v230, v231\n\t"
      "v_max3_u32 %[tw], %[tw], v232, v233\n\t"
      "v_max3_u32 %[tv], %[tv], v234, v235\n\t"
      "v_max3_u32 %[tw], %[tw], v236, v237\n\t"
      "v_max3_u32 %[tv], %[tv], v238, v239\n\t"
      "v_max3_u32 %[tw], %[tw], v240, v241\n\t"
      "v_max3_u32 %[tv], %[tv], v242, v243\n\t"
      "v_max3_u32 %[tw], %[tw], v244, v245\n\t"
      "v_max3_u32 %[tv], %[tv], v246, v247\n\t"
      "v_max3_u32 %[tw], %[tw], v248, v249\n\t"
      "v_max3_u32 %[tv], %[tv], v250, v251\n\t"
      "v_max3_u32 %[tw], %[tw], v252, v253\n\t"
      "v_max3_u32 %[tv], %[tv], v254, v255\n\t"
      "v_max_u32 %[tv], %[tv], %[tw]\n\t"
      "v_cmp_eq_u32 vcc, -1, %[tv]\n\t"
      "s_and_b64 vcc, vcc, vcc\n\t"
      "s_cbranch_scc0 10f\n\t"
      "s_mov_b64 %[any], 0\n\t"
      "v_max3_u32 %[tv], v224, v225, v226\n\t"
      "v_max_u32 %[tv], %[tv], v227\n\t"
      "v_cmp_eq_u32 vcc, -1, %[tv]\n\t"
      "s_and_b64 vcc, vcc, vcc\n\t"
      "s_cbranch_scc0 2f\n\t"
      "s_mov_b64 %[any], vcc\n\t"
      "s_mov_b64 exec, vcc\n\t"
      "buffer_load_dwordx4 v[224:227], %[vo], %[rs], 0 offen sc1\n\t"
      "s_mov_b64 exec, -1\n\t"
      "2:\n\t"
      "v_max3_u32 %[tv], v228, v229, v230\n\t"
      "v_max_u32 %[tv], %[tv], v231\n\t"
      "v_cmp_eq_u32 vcc, -1, %[tv]\n\t"
      "s_and_b64 vcc, vcc, vcc\n\t"
      "s_cbranch_scc0 3f\n\t"
      "s_mov_b64 %[any], vcc\n\t"
      "s_mov_b64 exec, vcc\n\t"
      "s_mov_b32 %[so], %c[st]*1\n\tbuffer_load_dwordx4 v[228:231], %[vo], %[rs], %[so] offen sc1\n\t"
      "s_mov_b64 exec, -1\n\t"
      "3:\n\t"
      "v_max3_u32 %[tv], v232, v233, v234\n\t"
      "v_max_u32 %[tv], %[tv], v235\n\t"
      "v_cmp_eq_u32 vcc, -1, %[tv]\n\t"
      "s_and_b64 vcc, vcc, vcc\n\t"
      "s_cbranch_scc0 4f\n\t"
      "s_mov_b64 %[any], vcc\n\t"
      "s_mov_b64 exec, vcc\n\t"
      "s_mov_b32 %[so], %c[st]*2\n\tbuffer_load_dwordx4 v[232:235], %[vo], %[rs], %[so] offen sc1\n\t"
      "s_mov_b64 exec, -1\n\t"
      "4:\n\t"
      "v_max3_u32 %[tv], v236, v237, v238\n\t"
      "v_max_u32 %[tv], %[tv], v239\n\t"
      "v_cmp_eq_u32 vcc, -1, %[tv]\n\t"
      "s_and_b64 vcc, vcc, vcc\n\t"
      "s_cbranch_scc0 5f\n\t"
      "s_mov_b64 %[any], vcc\n\t"
      "s_mov_b64 exec, vcc\n\t"
      "s_mov_b32 %[so], %c[st]*3\n\tbuffer_load_dwordx4 v[236:239], %[vo], %[rs], %[so] offen sc1\n\t"
      "s_mov_b64 exec, -1\n\t"
      "5:\n\t"
      "v_max3_u32 %[tv], v240, v241, v242\n\t"
      "v_max_u32 %[tv], %[tv], v243\n\t"
      "v_cmp_eq_u32 vcc, -1, %[tv]\n\t"
      "s_and_b64 vcc, vcc, vcc\n\t"
      "s_cbranch_scc0 6f\n\t"
      "s_mov_b64 %[any], vcc\n\t"
      "s_mov_b64 exec, vcc\n\t"
      "s_mov_b32 %[so], %c[st]*4\n\tbuffer_load_dwordx4 v[240:243], %[vo], %[rs], %[so] offen sc1\n\t"
      "s_mov_b64 exec, -1\n\t"
      "6:\n\t"
      "v_max3_u32 %[tv], v244, v245, v246\n\t"
      "v_max_u32 %[tv], %[tv], v247\n\t"
      "v_cmp_eq_u32 vcc, -1, %[tv]\n\t"
      "s_and_b64 vcc, vcc, vcc\n\t"
      "s_cbranch_scc0 7f\n\t"
      "s_mov_b64 %[any], vcc\n\t"
      "s_mov_b64 exec, vcc\n\t"
      "s_mov_b32 %[so], %c[st]*5\n\tbuffer_load_dwordx4 v[244:247], %[vo], %[rs], %[so] offen sc1\n\t"
      "s_mov_b64 exec, -1\n\t"
      "7:\n\t"
      "v_max3_u32 %[tv], v248, v249, v250\n\t"
      "v_max_u32 %[tv], %[tv], v251\n\t"
      "v_cmp_eq_u32 vcc, -1, %[tv]\n\t"
      "s_and_b64 vcc, vcc, vcc\n\t"
      "s_cbranch_scc0 8f\n\t"
      "s_mov_b64 %[any], vcc\n\t"
      "s_mov_b64 exec, vcc\n\t"
      "s_mov_b32 %[so], %c[st]*6\n\tbuffer_load_dwordx4 v[248:251], %[vo], %[rs], %[so] offen sc1\n\t"
      "s_mov_b64 exec, -1\n\t"
      "8:\n\t"
      "v_max3_u32 %[tv], v252, v253, v254\n\t"
      "v_max_u32 %[tv], %[tv], v255\n\t"
      "v_cmp_eq_u32 vcc, -1, %[tv]\n\t"
      "s_and_b64 vcc, vcc, vcc\n\t"
      "s_cbranch_scc0 9f\n\t"
      "s_mov_b64 %[any], vcc\n\t"
      "s_mov_b64 exec, vcc\n\t"
      "s_mov_b32 %[so], %c[st]*7\n\tbuffer_load_dwordx4 v[252:255], %[vo], %[rs], %[so] offen sc1\n\t"
      "s_mov_b64 exec, -1\n\t"
      "9:\n\t"
      "s_cmp_eq_u64 %[any], 0\n\t"
      "s_cbranch_scc1 10f\n\t"
      "s_sleep 2\n\t"
      "s_sub_u32 %[cnt], %[cnt], 1\n\t"
      "s_cmp_lg_u32 %[cnt], 0\n\t"
      "s_cbranch_scc1 1b\n\t"
      "s_waitcnt vmcnt(0)\n\t"
      "10:\n\t"
      : "+{v[224:227]}"(v[0]), "+{v[228:231]}"(v[1]), "+{v[232:235]}"(v[2]), "+{v[236:239]}"(v[3]), "+{v[240:243]}"(v[4]), "+{v[244:247]}"(v[5]), "+{v[248:251]}"(v[6]), "+{v[252:255]}"(v[7]), [so] "=&s"(so), [cnt] "=&s"(cnt), [tv] "=&v"(tv), [tw] "=&v"(tw), [t64] "=&s"(t64), [any] "=&s"(any)
      : [vo] "v"(voff), [rs] "s"(r), [st] "i"(STRIDE)
      : "vcc", "scc", "memory");
}
DI void c_rows0(LAS char* pe, LAS char* po, v32u& lo, v32u& hi) {
  asm volatile(
      "ds_read_b128 v[64:67], %[pe] offset:0\n\t"
      "ds_read_b128 v[68:71], %[po] offset:16\n\t"
      "ds_read_b128 v[72:75], %[pe] offset:32\n\t"
      "ds_read_b128 v[76:79], %[po] offset:48\n\t"
      "ds_read_b128 v[80:83], %[pe] offset:64\n\t"
      "ds_read_b128 v[84:87], %[po] offset:80\n\t"
      "ds_read_b128 v[88:91], %[pe] offset:96\n\t"
      "ds_read_b128 v[92:95], %[po] offset:112\n\t"
      "ds_read_b128 v[96:99], %[pe] offset:128\n\t"
      "ds_read_b128 v[100:103], %[po] offset:144\n\t"
      "ds_read_b128 v[104:107], %[pe] offset:160\n\t"
      "ds_read_b128 v[108:111], %[po] offset:176\n\t"
      "ds_read_b128 v[112:115], %[pe] offset:192\n\t"
      "ds_read_b128 v[116:119], %[po] offset:208\n\t"
      "ds_read_b128 v[120:123], %[pe] offset:224\n\t"
      "ds_read_b128 v[124:127], %[po] offset:240\n\t"
      : "=&{v[64:95]}"(lo), "=&{v[96:127]}"(hi) : [pe] "v"(pe), [po] "v"(po) : "memory");
}
DI void c_rows1(LAS char* pe, LAS char* po, v32u& lo, v32u& hi) {
  asm volatile(
      "ds_read_b128 v[128:131], %[pe] offset:16896\n\t"
      "ds_read_b128 v[132:135], %[po] offset:16912\n\t"
      "ds_read_b128 v[136:139], %[pe] offset:16928\n\t"
      "ds_read_b128 v[140:143], %[po] offset:16944\n\t"
      "ds_read_b128 v[144:147], %[pe] offset:16960\n\t"
      "ds_read_b128 v[148:151], %[po] offset:16976\n\t"
      "ds_read_b128 v[152:155], %[pe] offset:16992\n\t"
      "ds_read_b128 v[156:159], %[po] offset:17008\n\t"
      "ds_read_b128 v[160:163], %[pe] offset:17024\n\t"
      "ds_read_b128 v[164:167], %[po] offset:17040\n\t"
      "ds_read_b128 v[168:171], %[pe] offset:17056\n\t"
      "ds_read_b128 v[172:175], %[po] offset:17072\n\t"
      "ds_read_b128 v[176:179], %[pe] offset:17088\n\t"
      "ds_read_b128 v[180:183], %[po] offset:17104\n\t"
      "ds_read_b128 v[184:187], %[pe] offset:17120\n\t"
      "ds_read_b128 v[188:191], %[po] offset:17136\n\t"
      : "=&{v[128:159]}"(lo), "=&{v[160:191]}"(hi) : [pe] "v"(pe), [po] "v"(po) : "memory");
}
DI void scores_softmax(const v32u& c0, const v32u& c1, const v32u& c2, const v32u& c3, const v4u (&u)[8], LAS char* qr, LAS char* qr2, LAS char* qw2,
                       LAS char* mxw, LAS char* mxr, LAS char* pw, unsigned cqn) {
  unsigned st, cnt;
  asm volatile(
      "ds_read_b128 v[192:195], %[qr] offset:0\n\t"
      "ds_read_b128 v[196:199], %[qr] offset:1040\n\t"
      "ds_read_b128 v[200:203], %[qr] offset:2080\n\t"
      "ds_read_b128 v[204:207], %[qr] offset:3120\n\t"
      "ds_read_b128 v[208:211], %[qr] offset:4160\n\t"
      "ds_read_b128 v[212:215], %[qr] offset:5200\n\t"
      "ds_read_b128 v[216:219], %[qr] offset:6240\n\t"
      "ds_read_b128 v[220:223], %[qr] offset:7280\n\t"
      "s_mov_b64 exec, %[hi8]\n\t"
      "ds_write_b128 %[qw2], v[240:243] offset:0\n\t"
      "ds_write_b128 %[qw2], v[244:247] offset:64\n\t"
      "ds_write_b128 %[qw2], v[248:251] offset:128\n\t"
      "ds_write_b128 %[qw2], v[252:255] offset:192\n\t"
      "s_mov_b64 exec, -1\n\t"
      "s_waitcnt lgkmcnt(11)\n\t"
      "v_mfma_f32_16x16x32_bf16 v[32:35], v[64:67], v[192:195], 0\n\t"
      "v_mfma_f32_16x16x32_bf16 v[36:39], v[128:131], v[192:195], 0\n\t"
      "ds_read_b128 v[192:195], %[qr] offset:8320\n\t"
      "s_waitcnt lgkmcnt(11)\n\t"
      "v_mfma_f32_16x16x32_bf16 v[32:35], v[68:71], v[196:199], v[32:35]\n\t"
      "v_mfma_f32_16x16x32_bf16 v[36:39], v[132:135], v[196:199], v[36:39]\n\t"
      "ds_read_b128 v[196:199], %[qr] offset:9360\n\t"
      "s_waitcnt lgkmcnt(11)\n\t"
      "v_mfma_f32_16x16x32_bf16 v[32:35], v[72:75], v[200:203], v[32:35]\n\t"
      "v_mfma_f32_16x16x32_bf16 v[36:39], v[136:139], v[200:203], v[36:39]\n\t"
      "ds_read_b128 v[200:203], %[qr] offset:10400\n\t"
      "s_waitcnt lgkmcnt(11)\n\t"
      "v_mfma_f32_16x16x32_bf16 v[32:35], v[76:79], v[204:207], v[32:35]\n\t"
      "v_mfma_f32_16x16x32_bf16 v[36:39], v[140:143], v[204:207], v[36:39]\n\t"
      "s_waitcnt lgkmcnt(3)\n\t"
      "s_mov_b64 exec, 1\n\t"
      "ds_add_u32 %[cqn], %[one]\n\t"
      "s_mov_b64 exec, -1\n\t"
      "ds_read_b128 v[204:207], %[qr] offset:11440\n\t"
      "v_mfma_f32_16x16x32_bf16 v[32:35], v[80:83], v[208:211], v[32:35]\n\t"
      "v_mfma_f32_16x16x32_bf16 v[36:39], v[144:147], v[208:211], v[36:39]\n\t"
      "ds_read_b128 v[208:211], %[qr] offset:12480\n\t"
      "v_mfma_f32_16x16x32_bf16 v[32:35], v[84:87], v[212:215], v[32:35]\n\t"
      "v_mfma_f32_16x16x32_bf16 v[36:39], v[148:151], v[212:215], v[36:39]\n\t"
      "ds_read_b128 v[212:215], %[qr] offset:13520\n\t"
      "v_mfma_f32_16x16x32_bf16 v[32:35], v[88:91], v[216:219], v[32:35]\n\t"
      "v_mfma_f32_16x16x32_bf16 v[36:39], v[152:155], v[216:219], v[36:39]\n\t"
      "ds_read_b128 v[216:219], %[qr] offset:14560\n\t"
      "v_mfma_f32_16x16x32_bf16 v[32:35], v[92:95], v[220:223], v[32:35]\n\t"
      "v_mfma_f32_16x16x32_bf16 v[36:39], v[156:159], v[220:223], v[36:39]\n\t"
      "ds_read_b128 v[220:223], %[qr] offset:15600\n\t"
      "s_waitcnt lgkmcnt(8)\n\t"
      "v_mfma_f32_16x16x32_bf16 v[32:35], v[96:99], v[192:195], v[32:35]\n\t"
      "v_mfma_f32_16x16x32_bf16 v[36:39], v[160:163], v[192:195], v[36:39]\n\t"
      "ds_read_b128 v[192:195], %[qr] offset:16640\n\t"
      "s_waitcnt lgkmcnt(8)\n\t"
      "v_mfma_f32_16x16x32_bf16 v[32:35], v[100:103], v[196:199], v[32:35]\n\t"
      "v_mfma_f32_16x16x32_bf16 v[36:39], v[164:167], v[196:199], v[36:39]\n\t"
      "ds_read_b128 v[196:199], %[qr] offset:17680\n\t"
      "s_waitcnt lgkmcnt(8)\n\t"
      "v_mfma_f32_16x16x32_bf16 v[32:35], v[104:107], v[200:203], v[32:35]\n\t"
      "v_mfma_f32_16x16x32_bf16 v[36:39], v[168:171], v[200:203], v[36:39]\n\t"
      "ds_read_b128 v[200:203], %[qr] offset:18720\n\t"
      "s_waitcnt lgkmcnt(7)\n\t"
      "v_mfma_f32_16x16x32_bf16 v[32:35], v[108:111], v[204:207], v[32:35]\n\t"
      "v_mfma_f32_16x16x32_bf16 v[36:39], v[172:175], v[204:207], v[36:39]\n\t"
      "ds_read_b128 v[204:207], %[qr] offset:19760\n\t"
      "s_waitcnt lgkmcnt(7)\n\t"
      "v_mfma_f32_16x16x32_bf16 v[32:35], v[112:115], v[208:211], v[32:35]\n\t"
      "v_mfma_f32_16x16x32_bf16 v[36:39], v[176:179], v[208:211], v[36:39]\n\t"
      "ds_read_b32 v51, %[cqn]\n\t"
      "ds_read_b128 v[208:211], %[qr] offset:20800\n\t"
      "s_waitcnt lgkmcnt(8)\n\t"
      "v_mfma_f32_16x16x32_bf16 v[32:35], v[116:119], v[212:215], v[32:35]\n\t"
      "v_mfma_f32_16x16x32_bf16 v[36:39], v[180:183], v[212:215], v[36:39]\n\t"
      "ds_read_b128 v[212:215], %[qr] offset:21840\n\t"
      "s_waitcnt lgkmcnt(8)\n\t"
      "v_mfma_f32_16x16x32_bf16 v[32:35], v[120:123], v[216:219], v[32:35]\n\t"
      "v_mfma_f32_16x16x32_bf16 v[36:39], v[184:187], v[216:219], v[36:39]\n\t"
      "ds_read_b128 v[216:219], %[qr] offset:22880\n\t"
      "s_waitcnt lgkmcnt(8)\n\t"
      "v_mfma_f32_16x16x32_bf16 v[32:35], v[124:127], v[220:223], v[32:35]\n\t"
      "v_mfma_f32_16x16x32_bf16 v[36:39], v[188:191], v[220:223], v[36:39]\n\t"
      "s_waitcnt lgkmcnt(3)\n\t"
      "v_readfirstlane_b32 %[st], v51\n\t"
      "s_cmp_ge_u32 %[st], 4\n\t"
      "s_cbranch_scc1 2f\n\t"
      "s_mov_b32 %[cnt], 0x100000\n\t"
      "1:\n\t"
      "ds_read_b32 v51, %[cqn]\n\t"
      "s_waitcnt lgkmcnt(0)\n\t"
      "v_readfirstlane_b32 %[st], v51\n\t"
      "s_cmp_ge_u32 %[st], 4\n\t"
      "s_cbranch_scc1 2f\n\t"
      "s_sub_u32 %[cnt], %[cnt], 1\n\t"
      "s_cbranch_scc0 1b\n\t"
      "2:\n\t"
      "ds_read_b128 v[220:223], %[qr] offset:23920\n\t"
      "v_mfma_f32_16x16x32_bf16 v[40:43], v[64:67], v[192:195], 0\n\t"
      "v_mfma_f32_16x16x32_bf16 v[44:47], v[128:131], v[192:195], 0\n\t"
      "ds_read_b128 v[192:195], %[qr2] offset:0\n\t"
      "v_mfma_f32_16x16x32_bf16 v[40:43], v[68:71], v[196:199], v[40:43]\n\t"
      "v_mfma_f32_16x16x32_bf16 v[44:47], v[132:135], v[196:199], v[44:47]\n\t"
      "ds_read_b128 v[196:199], %[qr2] offset:1040\n\t"
      "v_mfma_f32_16x16x32_bf16 v[40:43], v[72:75], v[200:203], v[40:43]\n\t"
      "v_mfma_f32_16x16x32_bf16 v[44:47], v[136:139], v[200:203], v[44:47]\n\t"
      "ds_read_b128 v[200:203], %[qr2] offset:2080\n\t"
      "v_mfma_f32_16x16x32_bf16 v[40:43], v[76:79], v[204:207], v[40:43]\n\t"
      "v_maximum3_f32 v48, v32, v33, v34\n\t"
      "v_maximum3_f32 v49, v35, v36, v37\n\t"
      "v_mfma_f32_16x16x32_bf16 v[44:47], v[140:143], v[204:207], v[44:47]\n\t"
      "ds_read_b128 v[204:207], %[qr2] offset:3120\n\t"
      "v_maximum3_f32 v48, v48, v38, v39\n\t"
      "v_maximum3_f32 v48, v48, v49, v49\n\t"
      "s_waitcnt lgkmcnt(7)\n\t"
      "v_mfma_f32_16x16x32_bf16 v[40:43], v[80:83], v[208:211], v[40:43]\n\t"
      "v_mov_b32 v49, v48\n\t"
      "v_mfma_f32_16x16x32_bf16 v[44:47], v[144:147], v[208:211], v[44:47]\n\t"
      "ds_read_b128 v[208:211], %[qr2] offset:4160\n\t"
      "v_permlane16_swap_b32 v48, v49\n\t"
      "s_waitcnt lgkmcnt(7)\n\t"
      "v_mfma_f32_16x16x32_bf16 v[40:43], v[84:87], v[212:215], v[40:43]\n\t"
      "v_maximum3_f32 v48, v48, v49, v49\n\t"
      "ds_write_b32 %[mxw], v48\n\t"
      "v_mfma_f32_16x16x32_bf16 v[44:47], v[148:151], v[212:215], v[44:47]\n\t"
      "ds_read_b128 v[212:215], %[qr2] offset:5200\n\t"
      "s_waitcnt lgkmcnt(8)\n\t"
      "v_mfma_f32_16x16x32_bf16 v[40:43], v[88:91], v[216:219], v[40:43]\n\t"
      "v_mfma_f32_16x16x32_bf16 v[44:47], v[152:155], v[216:219], v[44:47]\n\t"
      "ds_read_b128 v[216:219], %[qr2] offset:6240\n\t"
      "s_waitcnt lgkmcnt(2)\n\t"
      "s_barrier\n\t"
      "ds_read_b128 v[52:55], %[mxr]\n\t"
      "ds_read_b128 v[56:59], %[mxr] offset:16\n\t"
      "v_mfma_f32_16x16x32_bf16 v[40:43], v[92:95], v[220:223], v[40:43]\n\t"
      "v_mfma_f32_16x16x32_bf16 v[44:47], v[156:159], v[220:223], v[44:47]\n\t"
      "ds_read_b128 v[220:223], %[qr2] offset:7280\n\t"
      "v_mfma_f32_16x16x32_bf16 v[40:43], v[96:99], v[192:195], v[40:43]\n\t"
      "v_mfma_f32_16x16x32_bf16 v[44:47], v[160:163], v[192:195], v[44:47]\n\t"
      "s_waitcnt lgkmcnt(1)\n\t"
      "v_mfma_f32_16x16x32_bf16 v[40:43], v[100:103], v[196:199], v[40:43]\n\t"
      "v_maximum3_f32 v48, v52, v53, v54\n\t"
      "v_maximum3_f32 v49, v55, v56, v57\n\t"
      "v_mfma_f32_16x16x32_bf16 v[44:47], v[164:167], v[196:199], v[44:47]\n\t"
      "v_maximum3_f32 v48, v48, v58, v59\n\t"
      "v_maximum3_f32 v50, v48, v49, v49\n\t"
      "v_mfma_f32_16x16x32_bf16 v[40:43], v[104:107], v[200:203], v[40:43]\n\t"
      "v_sub_f32 v32, v32, v50\n\t"
      "v_sub_f32 v33, v33, v50\n\t"
      "v_mfma_f32_16x16x32_bf16 v[44:47], v[168:171], v[200:203], v[44:47]\n\t"
      "v_sub_f32 v34, v34, v50\n\t"
      "v_sub_f32 v35, v35, v50\n\t"
      "v_mfma_f32_16x16x32_bf16 v[40:43], v[108:111], v[204:207], v[40:43]\n\t"
      "v_exp_f32 v32, v32\n\t"
      "v_sub_f32 v36, v36, v50\n\t"
      "v_mfma_f32_16x16x32_bf16 v[44:47], v[172:175], v[204:207], v[44:47]\n\t"
      "v_exp_f32 v33, v33\n\t"
      "v_sub_f32 v37, v37, v50\n\t"
      "v_mfma_f32_16x16x32_bf16 v[40:43], v[112:115], v[208:211], v[40:43]\n\t"
      "v_exp_f32 v34, v34\n\t"
      "v_sub_f32 v38, v38, v50\n\t"
      "v_mfma_f32_16x16x32_bf16 v[44:47], v[176:179], v[208:211], v[44:47]\n\t"
      "v_exp_f32 v35, v35\n\t"
      "v_sub_f32 v39, v39, v50\n\t"
      "v_mfma_f32_16x16x32_bf16 v[40:43], v[116:119], v[212:215], v[40:43]\n\t"
      "v_exp_f32 v36, v36\n\t"
      "v_cvt_pk_bf16_f32 v60, v32, v33\n\t"
      "v_mfma_f32_16x16x32_bf16 v[44:47], v[180:183], v[212:215], v[44:47]\n\t"
      "v_exp_f32 v37, v37\n\t"
      "v_cvt_pk_bf16_f32 v61, v34, v35\n\t"
      "v_mfma_f32_16x16x32_bf16 v[40:43], v[120:123], v[216:219], v[40:43]\n\t"
      "v_exp_f32 v38, v38\n\t"
      "v_mfma_f32_16x16x32_bf16 v[44:47], v[184:187], v[216:219], v[44:47]\n\t"
      "v_exp_f32 v39, v39\n\t"
      "v_cvt_pk_bf16_f32 v62, v36, v37\n\t"
      "s_waitcnt lgkmcnt(0)\n\t"
      "v_mfma_f32_16x16x32_bf16 v[40:43], v[124:127], v[220:223], v[40:43]\n\t"
      "v_cvt_pk_bf16_f32 v63, v38, v39\n\t"
      "ds_write_b128 %[pw], v[60:63]\n\t"
      "v_mfma_f32_16x16x32_bf16 v[44:47], v[188:191], v[220:223], v[44:47]\n\t"
      "s_waitcnt lgkmcnt(0)\n\t"
      "s_barrier\n\t"
      "s_nop 4\n\t"
      "v_maximum3_f32 v48, v40, v41, v42\n\t"
      "v_maximum3_f32 v49, v43, v44, v45\n\t"
      "v_maximum3_f32 v48, v48, v46, v47\n\t"
      "v_maximum3_f32 v48, v48, v49, v49\n\t"
      "v_mov_b32 v49, v48\n\t"
      "s_nop 1\n\t"
      "v_permlane16_swap_b32 v48, v49\n\t"
      "s_nop 0\n\t"
      "v_maximum3_f32 v48, v48, v49, v49\n\t"
      "ds_write_b32 %[mxw], v48 offset:512\n\t"
      "s_mov_b64 exec, 1\n\t"
      "ds_add_u32 %[cqn], %[one]\n\t"
      "s_mov_b64 exec, -1\n\t"
      "s_mov_b32 %[cnt], 0x100000\n\t"
      "3:\n\t"
      "ds_read_b32 v51, %[cqn]\n\t"
      "ds_read_b128 v[52:55], %[mxr] offset:512\n\t"
      "ds_read_b128 v[56:59], %[mxr] offset:528\n\t"
      "s_waitcnt lgkmcnt(0)\n\t"
      "v_readfirstlane_b32 %[st], v51\n\t"
      "s_cmp_ge_u32 %[st], 8\n\t"
      "s_cbranch_scc1 4f\n\t"
      "s_sub_u32 %[cnt], %[cnt], 1\n\t"
      "s_cbranch_scc0 3b\n\t"
      "4:\n\t"
      "v_maximum3_f32 v48, v52, v53, v54\n\t"
      "v_maximum3_f32 v49, v55, v56, v57\n\t"
      "v_maximum3_f32 v48, v48, v58, v59\n\t"
      "v_maximum3_f32 v50, v48, v49, v49\n\t"
      "v_sub_f32 v40, v40, v50\n\t"
      "v_sub_f32 v41, v41, v50\n\t"
      "v_sub_f32 v42, v42, v50\n\t"
      "v_sub_f32 v43, v43, v50\n\t"
      "v_sub_f32 v44, v44, v50\n\t"
      "v_sub_f32 v45, v45, v50\n\t"
      "v_sub_f32 v46, v46, v50\n\t"
      "v_sub_f32 v47, v47, v50\n\t"
      "v_exp_f32 v40, v40\n\t"
      "v_exp_f32 v41, v41\n\t"
      "v_exp_f32 v42, v42\n\t"
      "v_exp_f32 v43, v43\n\t"
      "v_exp_f32 v44, v44\n\t"
      "v_exp_f32 v45, v45\n\t"
      "v_exp_f32 v46, v46\n\t"
      "v_exp_f32 v47, v47\n\t"
      "s_nop 0\n\t"
      "v_cvt_pk_bf16_f32 v60, v40, v41\n\t"
      "v_cvt_pk_bf16_f32 v61, v42, v43\n\t"
      "v_cvt_pk_bf16_f32 v62, v44, v45\n\t"
      "v_cvt_pk_bf16_f32 v63, v46, v47\n\t"
      "ds_write_b128 %[pw], v[60:63] offset:4096\n\t"
      "s_waitcnt lgkmcnt(0)\n\t"
      "s_barrier\n\t"
      : [st] "=&s"(st), [cnt] "=&s"(cnt)
      : "{v[64:95]}"(c0), "{v[96:127]}"(c1), "{v[128:159]}"(c2), "{v[160:191]}"(c3), "{v[240:243]}"(u[4]), "{v[244:247]}"(u[5]), "{v[248:251]}"(u[6]), "{v[252:255]}"(u[7]),
        [qr] "v"(qr), [qr2] "v"(qr2), [qw2] "v"(qw2), [mxw] "v"(mxw), [mxr] "v"(mxr), [pw] "v"(pw), [cqn] "v"(cqn), [one] "v"(1u), [hi8] "s"(0xff00ff00ff00ff00ull)
      : "scc", "memory", "v32", "v33", "v34", "v35", "v36", "v37", "v38", "v39", "v40", "v41", "v42", "v43", "v44", "v45", "v46", "v47", "v48", "v49", "v50", "v51", "v52", "v53", "v54", "v55", "v56", "v57", "v58", "v59", "v60", "v61", "v62", "v63", "v192", "v193", "v194", "v195", "v196", "v197", "v198", "v199", "v200", "v201", "v202", "v203", "v204", "v205", "v206", "v207", "v208", "v209", "v210", "v211", "v212", "v213", "v214", "v215", "v216", "v217", "v218", "v219", "v220", "v221", "v222", "v223");
}
DI void c_cols(LAS char* c0, LAS char* c1, LAS char* c2, v32u& t0, v32u& t1, v32u& t2, v32u& t3) {
  asm volatile(
      "ds_read_b64_tr_b16 v[64:65], %[c0] offset:0\n\t"
      "ds_read_b64_tr_b16 v[66:67], %[c0] offset:16896\n\t"
      "ds_read_b64_tr_b16 v[68:69], %[c0] offset:33792\n\t"
      "ds_read_b64_tr_b16 v[70:71], %[c1] offset:0\n\t"
      "ds_read_b64_tr_b16 v[72:73], %[c1] offset:16896\n\t"
      "ds_read_b64_tr_b16 v[74:75], %[c1] offset:33792\n\t"
      "ds_read_b64_tr_b16 v[76:77], %[c2] offset:0\n\t"
      "ds_read_b64_tr_b16 v[78:79], %[c2] offset:16896\n\t"
      "ds_read_b64_tr_b16 v[80:81], %[c0] offset:32\n\t"
      "ds_read_b64_tr_b16 v[82:83], %[c0] offset:16928\n\t"
      "ds_read_b64_tr_b16 v[84:85], %[c0] offset:33824\n\t"
      "ds_read_b64_tr_b16 v[86:87], %[c1] offset:32\n\t"
      "ds_read_b64_tr_b16 v[88:89], %[c1] offset:16928\n\t"
      "ds_read_b64_tr_b16 v[90:91], %[c1] offset:33824\n\t"
      "ds_read_b64_tr_b16 v[92:93], %[c2] offset:32\n\t"
      "ds_read_b64_tr_b16 v[94:95], %[c2] offset:16928\n\t"
      "ds_read_b64_tr_b16 v[96:97], %[c0] offset:64\n\t"
      "ds_read_b64_tr_b16 v[98:99], %[c0] offset:16960\n\t"
      "ds_read_b64_tr_b16 v[100:101], %[c0] offset:33856\n\t"
      "ds_read_b64_tr_b16 v[102:103], %[c1] offset:64\n\t"
      "ds_read_b64_tr_b16 v[104:105], %[c1] offset:16960\n\t"
      "ds_read_b64_tr_b16 v[106:107], %[c1] offset:33856\n\t"
      "ds_read_b64_tr_b16 v[108:109], %[c2] offset:64\n\t"
      "ds_read_b64_tr_b16 v[110:111], %[c2] offset:16960\n\t"
      "ds_read_b64_tr_b16 v[112:113], %[c0] offset:96\n\t"
      "ds_read_b64_tr_b16 v[114:115], %[c0] offset:16992\n\t"
      "ds_read_b64_tr_b16 v[116:117], %[c0] offset:33888\n\t"
      "ds_read_b64_tr_b16 v[118:119], %[c1] offset:96\n\t"
      "ds_read_b64_tr_b16 v[120:121], %[c1] offset:16992\n\t"
      "ds_read_b64_tr_b16 v[122:123], %[c1] offset:33888\n\t"
      "ds_read_b64_tr_b16 v[124:125], %[c2] offset:96\n\t"
      "ds_read_b64_tr_b16 v[126:127], %[c2] offset:16992\n\t"
      "ds_read_b64_tr_b16 v[128:129], %[c0] offset:128\n\t"
      "ds_read_b64_tr_b16 v[130:131], %[c0] offset:17024\n\t"
      "ds_read_b64_tr_b16 v[132:133], %[c0] offset:33920\n\t"
      "ds_read_b64_tr_b16 v[134:135], %[c1] offset:128\n\t"
      "ds_read_b64_tr_b16 v[136:137], %[c1] offset:17024\n\t"
      "ds_read_b64_tr_b16 v[138:139], %[c1] offset:33920\n\t"
      "ds_read_b64_tr_b16 v[140:141], %[c2] offset:128\n\t"
      "ds_read_b64_tr_b16 v[142:143], %[c2] offset:17024\n\t"
      "ds_read_b64_tr_b16 v[144:145], %[c0] offset:160\n\t"
      "ds_read_b64_tr_b16 v[146:147], %[c0] offset:17056\n\t"
      "ds_read_b64_tr_b16 v[148:149], %[c0] offset:33952\n\t"
      "ds_read_b64_tr_b16 v[150:151], %[c1] offset:160\n\t"
      "ds_read_b64_tr_b16 v[152:153], %[c1] offset:17056\n\t"
      "ds_read_b64_tr_b16 v[154:155], %[c1] offset:33952\n\t"
      "ds_read_b64_tr_b16 v[156:157], %[c2] offset:160\n\t"
      "ds_read_b64_tr_b16 v[158:159], %[c2] offset:17056\n\t"
      "ds_read_b64_tr_b16 v[160:161], %[c0] offset:192\n\t"
      "ds_read_b64_tr_b16 v[162:163], %[c0] offset:17088\n\t"
      "ds_read_b64_tr_b16 v[164:165], %[c0] offset:33984\n\t"
      "ds_read_b64_tr_b16 v[166:167], %[c1] offset:192\n\t"
      "ds_read_b64_tr_b16 v[168:169], %[c1] offset:17088\n\t"
      "ds_read_b64_tr_b16 v[170:171], %[c1] offset:33984\n\t"
      "ds_read_b64_tr_b16 v[172:173], %[c2] offset:192\n\t"
      "ds_read_b64_tr_b16 v[174:175], %[c2] offset:17088\n\t"
      "ds_read_b64_tr_b16 v[176:177], %[c0] offset:224\n\t"
      "ds_read_b64_tr_b16 v[178:179], %[c0] offset:17120\n\t"
      "ds_read_b64_tr_b16 v[180:181], %[c0] offset:34016\n\t"
      "ds_read_b64_tr_b16 v[182:183], %[c1] offset:224\n\t"
      "ds_read_b64_tr_b16 v[184:185], %[c1] offset:17120\n\t"
      "ds_read_b64_tr_b16 v[186:187], %[c1] offset:34016\n\t"
      "ds_read_b64_tr_b16 v[188:189], %[c2] offset:224\n\t"
      "ds_read_b64_tr_b16 v[190:191], %[c2] offset:17120\n\t"
      : "=&{v[64:95]}"(t0), "=&{v[96:127]}"(t1), "=&{v[128:159]}"(t2), "=&{v[160:191]}"(t3) : [c0] "v"(c0), [c1] "v"(c1), [c2] "v"(c2) : "memory");
}
DI void pv_publish(const v32u& t0, const v32u& t1, const v32u& t2, const v32u& t3, LAS char* pr, LAS char* mxr, rsrc_t rs, unsigned ol, unsigned fo, unsigned tag) {
  asm volatile(
      "v_mov_b32 v248, 0x3f803f80\n\t"
      "v_mov_b32 v249, 0x3f803f80\n\t"
      "v_mov_b32 v250, 0x3f803f80\n\t"
      "v_mov_b32 v251, 0x3f803f80\n\t"
      "v_mov_b32 v28, 0\n\t"
      "v_mov_b32 v29, 0\n\t"
      "v_mov_b32 v30, 0\n\t"
      "v_mov_b32 v31, 0\n\t"
      "s_barrier\n\t"
      "s_barrier\n\t"
      "ds_read_b128 v[192:195], %[pr] offset:0\n\t"
      "ds_read_b128 v[196:199], %[pr] offset:1024\n\t"
      "ds_read_b128 v[200:203], %[pr] offset:2048\n\t"
      "ds_read_b128 v[204:207], %[pr] offset:3072\n\t"
      "s_waitcnt lgkmcnt(3)\n\t"
      "v_mfma_f32_16x16x32_bf16 v[32:35], v[64:67], v[192:195], 0\n\t"
      "v_mfma_f32_16x16x32_bf16 v[36:39], v[80:83], v[192:195], 0\n\t"
      "v_mfma_f32_16x16x32_bf16 v[40:43], v[96:99], v[192:195], 0\n\t"
      "v_mfma_f32_16x16x32_bf16 v[44:47], v[112:115], v[192:195], 0\n\t"
      "s_waitcnt lgkmcnt(2)\n\t"
      "v_mfma_f32_16x16x32_bf16 v[32:35], v[68:71], v[196:199], v[32:35]\n\t"
      "v_mfma_f32_16x16x32_bf16 v[36:39], v[84:87], v[196:199], v[36:39]\n\t"
      "v_mfma_f32_16x16x32_bf16 v[40:43], v[100:103], v[196:199], v[40:43]\n\t"
      "v_mfma_f32_16x16x32_bf16 v[44:47], v[116:119], v[196:199], v[44:47]\n\t"
      "s_waitcnt lgkmcnt(1)\n\t"
      "v_mfma_f32_16x16x32_bf16 v[32:35], v[72:75], v[200:203], v[32:35]\n\t"
      "v_mfma_f32_16x16x32_bf16 v[36:39], v[88:91], v[200:203], v[36:39]\n\t"
      "v_mfma_f32_16x16x32_bf16 v[40:43], v[104:107], v[200:203], v[40:43]\n\t"
      "v_mfma_f32_16x16x32_bf16 v[44:47], v[120:123], v[200:203], v[44:47]\n\t"
      "s_waitcnt lgkmcnt(0)\n\t"
      "v_mfma_f32_16x16x32_bf16 v[32:35], v[76:79], v[204:207], v[32:35]\n\t"
      "v_mfma_f32_16x16x32_bf16 v[36:39], v[92:95], v[204:207], v[36:39]\n\t"
      "v_mfma_f32_16x16x32_bf16 v[40:43], v[108:111], v[204:207], v[40:43]\n\t"
      "v_mfma_f32_16x16x32_bf16 v[44:47], v[124:127], v[204:207], v[44:47]\n\t"
      "v_mfma_f32_16x16x32_bf16 v[48:51], v[128:131], v[192:195], 0\n\t"
      "v_mfma_f32_16x16x32_bf16 v[52:55], v[144:147], v[192:195], 0\n\t"
      "v_mfma_f32_16x16x32_bf16 v[56:59], v[160:163], v[192:195], 0\n\t"
      "v_mfma_f32_16x16x32_bf16 v[60:63], v[176:179], v[192:195], 0\n\t"
      "v_mfma_f32_16x16x32_bf16 v[48:51], v[132:135], v[196:199], v[48:51]\n\t"
      "v_cvt_pk_bf16_f32 v224, v32, v33\n\t"
      "v_cvt_pk_bf16_f32 v225, v34, v35\n\t"
      "v_cvt_pk_bf16_f32 v226, v36, v37\n\t"
      "v_mfma_f32_16x16x32_bf16 v[52:55], v[148:151], v[196:199], v[52:55]\n\t"
      "v_cvt_pk_bf16_f32 v227, v38, v39\n\t"
      "v_cvt_pk_bf16_f32 v228, v40, v41\n\t"
      "v_cvt_pk_bf16_f32 v229, v42, v43\n\t"
      "v_mfma_f32_16x16x32_bf16 v[56:59], v[164:167], v[196:199], v[56:59]\n\t"
      "v_cvt_pk_bf16_f32 v230, v44, v45\n\t"
      "v_cvt_pk_bf16_f32 v231, v46, v47\n\t"
      "s_mov_b64 vcc, %[lo8]\n\t"
      "v_cndmask_b32_dpp v232, v228, v224, vcc row_ror:8 row_mask:0xf bank_mask:0xf\n\t"
      "v_mfma_f32_16x16x32_bf16 v[60:63], v[180:183], v[196:199], v[60:63]\n\t"
      "v_cndmask_b32_dpp v233, v229, v225, vcc row_ror:8 row_mask:0xf bank_mask:0xf\n\t"
      "v_cndmask_b32_dpp v234, v230, v226, vcc row_ror:8 row_mask:0xf bank_mask:0xf\n\t"
      "v_mfma_f32_16x16x32_bf16 v[48:51], v[136:139], v[200:203], v[48:51]\n\t"
      "v_cndmask_b32_dpp v235, v231, v227, vcc row_ror:8 row_mask:0xf bank_mask:0xf\n\t"
      "s_not_b64 vcc, vcc\n\t"
      "buffer_store_dwordx4 v[232:235], %[ol], %[rs], %[s0] offen offset:0 sc1\n\t"
      "v_mfma_f32_16x16x32_bf16 v[52:55], v[152:155], v[200:203], v[52:55]\n\t"
      "v_cndmask_b32_dpp v236, v224, v228, vcc row_ror:8 row_mask:0xf bank_mask:0xf\n\t"
      "v_cndmask_b32_dpp v237, v225, v229, vcc row_ror:8 row_mask:0xf bank_mask:0xf\n\t"
      "v_mfma_f32_16x16x32_bf16 v[56:59], v[168:171], v[200:203], v[56:59]\n\t"
      "v_cndmask_b32_dpp v238, v226, v230, vcc row_ror:8 row_mask:0xf bank_mask:0xf\n\t"
      "v_cndmask_b32_dpp v239, v227, v231, vcc row_ror:8 row_mask:0xf bank_mask:0xf\n\t"
      "v_mfma_f32_16x16x32_bf16 v[60:63], v[184:187], v[200:203], v[60:63]\n\t"
      "buffer_store_dwordx4 v[236:239], %[ol], %[rs], %[s1] offen offset:0 sc1\n\t"
      "v_mfma_f32_16x16x32_bf16 v[48:51], v[140:143], v[204:207], v[48:51]\n\t"
      "v_add3_u32 v28, v28, v224, v225\n\t"
      "v_bitop3_b32 v30, v30, v224, v225 bitop3:0x96\n\t"
      "v_mfma_f32_16x16x32_bf16 v[52:55], v[156:159], v[204:207], v[52:55]\n\t"
      "v_add3_u32 v28, v28, v226, v227\n\t"
      "v_bitop3_b32 v30, v30, v226, v227 bitop3:0x96\n\t"
      "v_mfma_f32_16x16x32_bf16 v[56:59], v[172:175], v[204:207], v[56:59]\n\t"
      "v_add3_u32 v28, v28, v228, v229\n\t"
      "v_bitop3_b32 v30, v30, v228, v229 bitop3:0x96\n\t"
      "v_mfma_f32_16x16x32_bf16 v[60:63], v[188:191], v[204:207], v[60:63]\n\t"
      "v_add3_u32 v28, v28, v230, v231\n\t"
      "v_bitop3_b32 v30, v30, v230, v231 bitop3:0x96\n\t"
      "s_barrier\n\t"
      "ds_read_b128 v[208:211], %[pr] offset:4096\n\t"
      "ds_read_b128 v[212:215], %[pr] offset:5120\n\t"
      "ds_read_b128 v[216:219], %[pr] offset:6144\n\t"
      "ds_read_b128 v[220:223], %[pr] offset:7168\n\t"
      "ds_read_b128 v[240:243], %[mxr]\n\t"
      "ds_read_b128 v[244:247], %[mxr] offset:16\n\t"
      "s_nop 1\n\t"
      "v_cvt_pk_bf16_f32 v224, v48, v49\n\t"
      "v_cvt_pk_bf16_f32 v225, v50, v51\n\t"
      "v_cvt_pk_bf16_f32 v226, v52, v53\n\t"
      "v_cvt_pk_bf16_f32 v227, v54, v55\n\t"
      "v_cvt_pk_bf16_f32 v228, v56, v57\n\t"
      "v_cvt_pk_bf16_f32 v229, v58, v59\n\t"
      "v_cvt_pk_bf16_f32 v230, v60, v61\n\t"
      "v_cvt_pk_bf16_f32 v231, v62, v63\n\t"
      "s_waitcnt lgkmcnt(5)\n\t"
      "v_mfma_f32_16x16x32_bf16 v[32:35], v[64:67], v[208:211], 0\n\t"
      "s_mov_b64 vcc, %[lo8]\n\t"
      "v_cndmask_b32_dpp v232, v228, v224, vcc row_ror:8 row_mask:0xf bank_mask:0xf\n\t"
      "v_cndmask_b32_dpp v233, v229, v225, vcc row_ror:8 row_mask:0xf bank_mask:0xf\n\t"
      "v_mfma_f32_16x16x32_bf16 v[36:39], v[80:83], v[208:211], 0\n\t"
      "v_cndmask_b32_dpp v234, v230, v226, vcc row_ror:8 row_mask:0xf bank_mask:0xf\n\t"
      "v_cndmask_b32_dpp v235, v231, v227, vcc row_ror:8 row_mask:0xf bank_mask:0xf\n\t"
      "v_mfma_f32_16x16x32_bf16 v[40:43], v[96:99], v[208:211], 0\n\t"
      "s_not_b64 vcc, vcc\n\t"
      "buffer_store_dwordx4 v[232:235], %[ol], %[rs], %[s0] offen offset:128 sc1\n\t"
      "v_mfma_f32_16x16x32_bf16 v[44:47], v[112:115], v[208:211], 0\n\t"
      "v_cndmask_b32_dpp v236, v224, v228, vcc row_ror:8 row_mask:0xf bank_mask:0xf\n\t"
      "v_cndmask_b32_dpp v237, v225, v229, vcc row_ror:8 row_mask:0xf bank_mask:0xf\n\t"
      "s_waitcnt lgkmcnt(4)\n\t"
      "v_mfma_f32_16x16x32_bf16 v[32:35], v[68:71], v[212:215], v[32:35]\n\t"
      "v_cndmask_b32_dpp v238, v226, v230, vcc row_ror:8 row_mask:0xf bank_mask:0xf\n\t"
      "v_cndmask_b32_dpp v239, v227, v231, vcc row_ror:8 row_mask:0xf bank_mask:0xf\n\t"
      "v_mfma_f32_16x16x32_bf16 v[36:39], v[84:87], v[212:215], v[36:39]\n\t"
      "buffer_store_dwordx4 v[236:239], %[ol], %[rs], %[s1] offen offset:128 sc1\n\t"
      "v_mfma_f32_16x16x32_bf16 v[40:43], v[100:103], v[212:215], v[40:43]\n\t"
      "v_add3_u32 v28, v28, v224, v225\n\t"
      "v_bitop3_b32 v30, v30, v224, v225 bitop3:0x96\n\t"
      "v_mfma_f32_16x16x32_bf16 v[44:47], v[116:119], v[212:215], v[44:47]\n\t"
      "v_add3_u32 v28, v28, v226, v227\n\t"
      "v_bitop3_b32 v30, v30, v226, v227 bitop3:0x96\n\t"
      "s_waitcnt lgkmcnt(3)\n\t"
      "v_mfma_f32_16x16x32_bf16 v[32:35], v[72:75], v[216:219], v[32:35]\n\t"
      "v_add3_u32 v28, v28, v228, v229\n\t"
      "v_bitop3_b32 v30, v30, v228, v229 bitop3:0x96\n\t"
      "v_mfma_f32_16x16x32_bf16 v[36:39], v[88:91], v[216:219], v[36:39]\n\t"
      "v_add3_u32 v28, v28, v230, v231\n\t"
      "v_bitop3_b32 v30, v30, v230, v231 bitop3:0x96\n\t"
      "v_mfma_f32_16x16x32_bf16 v[40:43], v[104:107], v[216:219], v[40:43]\n\t"
      "v_mfma_f32_16x16x32_bf16 v[44:47], v[120:123], v[216:219], v[44:47]\n\t"
      "s_waitcnt lgkmcnt(2)\n\t"
      "v_mfma_f32_16x16x32_bf16 v[32:35], v[76:79], v[220:223], v[32:35]\n\t"
      "v_mfma_f32_16x16x32_bf16 v[36:39], v[92:95], v[220:223], v[36:39]\n\t"
      "v_mfma_f32_16x16x32_bf16 v[40:43], v[108:111], v[220:223], v[40:43]\n\t"
      "v_mfma_f32_16x16x32_bf16 v[44:47], v[124:127], v[220:223], v[44:47]\n\t"
      "v_mfma_f32_16x16x32_bf16 v[48:51], v[128:131], v[208:211], 0\n\t"
      "v_mfma_f32_16x16x32_bf16 v[52:55], v[144:147], v[208:211], 0\n\t"
      "v_mfma_f32_16x16x32_bf16 v[56:59], v[160:163], v[208:211], 0\n\t"
      "v_mfma_f32_16x16x32_bf16 v[60:63], v[176:179], v[208:211], 0\n\t"
      "v_mfma_f32_16x16x32_bf16 v[48:51], v[132:135], v[212:215], v[48:51]\n\t"
      "v_cvt_pk_bf16_f32 v224, v32, v33\n\t"
      "v_cvt_pk_bf16_f32 v225, v34, v35\n\t"
      "v_cvt_pk_bf16_f32 v226, v36, v37\n\t"
      "v_mfma_f32_16x16x32_bf16 v[52:55], v[148:151], v[212:215], v[52:55]\n\t"
      "v_cvt_pk_bf16_f32 v227, v38, v39\n\t"
      "v_cvt_pk_bf16_f32 v228, v40, v41\n\t"
      "v_cvt_pk_bf16_f32 v229, v42, v43\n\t"
      "v_mfma_f32_16x16x32_bf16 v[56:59], v[164:167], v[212:215], v[56:59]\n\t"
      "v_cvt_pk_bf16_f32 v230, v44, v45\n\t"
      "v_cvt_pk_bf16_f32 v231, v46, v47\n\t"
      "s_mov_b64 vcc, %[lo8]\n\t"
      "v_cndmask_b32_dpp v232, v228, v224, vcc row_ror:8 row_mask:0xf bank_mask:0xf\n\t"
      "v_mfma_f32_16x16x32_bf16 v[60:63], v[180:183], v[212:215], v[60:63]\n\t"
      "v_cndmask_b32_dpp v233, v229, v225, vcc row_ror:8 row_mask:0xf bank_mask:0xf\n\t"
      "v_cndmask_b32_dpp v234, v230, v226, vcc row_ror:8 row_mask:0xf bank_mask:0xf\n\t"
      "v_mfma_f32_16x16x32_bf16 v[48:51], v[136:139], v[216:219], v[48:51]\n\t"
      "v_cndmask_b32_dpp v235, v231, v227, vcc row_ror:8 row_mask:0xf bank_mask:0xf\n\t"
      "s_not_b64 vcc, vcc\n\t"
      "buffer_store_dwordx4 v[232:235], %[ol], %[rs], %[s2] offen offset:0 sc1\n\t"
      "v_mfma_f32_16x16x32_bf16 v[52:55], v[152:155], v[216:219], v[52:55]\n\t"
      "v_cndmask_b32_dpp v236, v224, v228, vcc row_ror:8 row_mask:0xf bank_mask:0xf\n\t"
      "v_cndmask_b32_dpp v237, v225, v229, vcc row_ror:8 row_mask:0xf bank_mask:0xf\n\t"
      "v_mfma_f32_16x16x32_bf16 v[56:59], v[168:171], v[216:219], v[56:59]\n\t"
      "v_cndmask_b32_dpp v238, v226, v230, vcc row_ror:8 row_mask:0xf bank_mask:0xf\n\t"
      "v_cndmask_b32_dpp v239, v227, v231, vcc row_ror:8 row_mask:0xf bank_mask:0xf\n\t"
      "v_mfma_f32_16x16x32_bf16 v[60:63], v[184:187], v[216:219], v[60:63]\n\t"
      "buffer_store_dwordx4 v[236:239], %[ol], %[rs], %[s3] offen offset:0 sc1\n\t"
      "v_mfma_f32_16x16x32_bf16 v[48:51], v[140:143], v[220:223], v[48:51]\n\t"
      "v_add3_u32 v29, v29, v224, v225\n\t"
      "v_bitop3_b32 v31, v31, v224, v225 bitop3:0x96\n\t"
      "v_mfma_f32_16x16x32_bf16 v[52:55], v[156:159], v[220:223], v[52:55]\n\t"
      "v_add3_u32 v29, v29, v226, v227\n\t"
      "v_bitop3_b32 v31, v31, v226, v227 bitop3:0x96\n\t"
      "v_mfma_f32_16x16x32_bf16 v[56:59], v[172:175], v[220:223], v[56:59]\n\t"
      "v_add3_u32 v29, v29, v228, v229\n\t"
      "v_bitop3_b32 v31, v31, v228, v229 bitop3:0x96\n\t"
      "v_mfma_f32_16x16x32_bf16 v[60:63], v[188:191], v[220:223], v[60:63]\n\t"
      "v_add3_u32 v29, v29, v230, v231\n\t"
      "v_bitop3_b32 v31, v31, v230, v231 bitop3:0x96\n\t"
      "s_nop 4\n\t"
      "v_cvt_pk_bf16_f32 v224, v48, v49\n\t"
      "v_cvt_pk_bf16_f32 v225, v50, v51\n\t"
      "v_cvt_pk_bf16_f32 v226, v52, v53\n\t"
      "v_cvt_pk_bf16_f32 v227, v54, v55\n\t"
      "v_cvt_pk_bf16_f32 v228, v56, v57\n\t"
      "v_cvt_pk_bf16_f32 v229, v58, v59\n\t"
      "v_cvt_pk_bf16_f32 v230, v60, v61\n\t"
      "v_cvt_pk_bf16_f32 v231, v62, v63\n\t"
      "s_mov_b64 vcc, %[lo8]\n\t"
      "v_cndmask_b32_dpp v232, v228, v224, vcc row_ror:8 row_mask:0xf bank_mask:0xf\n\t"
      "v_cndmask_b32_dpp v233, v229, v225, vcc row_ror:8 row_mask:0xf bank_mask:0xf\n\t"
      "v_cndmask_b32_dpp v234, v230, v226, vcc row_ror:8 row_mask:0xf bank_mask:0xf\n\t"
      "v_cndmask_b32_dpp v235, v231, v227, vcc row_ror:8 row_mask:0xf bank_mask:0xf\n\t"
      "s_not_b64 vcc, vcc\n\t"
      "buffer_store_dwordx4 v[232:235], %[ol], %[rs], %[s2] offen offset:128 sc1\n\t"
      "v_cndmask_b32_dpp v236, v224, v228, vcc row_ror:8 row_mask:0xf bank_mask:0xf\n\t"
      "v_cndmask_b32_dpp v237, v225, v229, vcc row_ror:8 row_mask:0xf bank_mask:0xf\n\t"
      "v_cndmask_b32_dpp v238, v226, v230, vcc row_ror:8 row_mask:0xf bank_mask:0xf\n\t"
      "v_cndmask_b32_dpp v239, v227, v231, vcc row_ror:8 row_mask:0xf bank_mask:0xf\n\t"
      "buffer_store_dwordx4 v[236:239], %[ol], %[rs], %[s3] offen offset:128 sc1\n\t"
      "v_mfma_f32_16x16x32_bf16 v[32:35], v[248:251], v[192:195], 0\n\t"
      "v_add3_u32 v29, v29, v224, v225\n\t"
      "v_mfma_f32_16x16x32_bf16 v[36:39], v[248:251], v[208:211], 0\n\t"
      "v_bitop3_b32 v31, v31, v224, v225 bitop3:0x96\n\t"
      "v_mfma_f32_16x16x32_bf16 v[32:35], v[248:251], v[196:199], v[32:35]\n\t"
      "v_add3_u32 v29, v29, v226, v227\n\t"
      "v_mfma_f32_16x16x32_bf16 v[36:39], v[248:251], v[212:215], v[36:39]\n\t"
      "v_bitop3_b32 v31, v31, v226, v227 bitop3:0x96\n\t"
      "v_mfma_f32_16x16x32_bf16 v[32:35], v[248:251], v[200:203], v[32:35]\n\t"
      "v_add3_u32 v29, v29, v228, v229\n\t"
      "v_mfma_f32_16x16x32_bf16 v[36:39], v[248:251], v[216:219], v[36:39]\n\t"
      "v_bitop3_b32 v31, v31, v228, v229 bitop3:0x96\n\t"
      "v_mfma_f32_16x16x32_bf16 v[32:35], v[248:251], v[204:207], v[32:35]\n\t"
      "v_add3_u32 v29, v29, v230, v231\n\t"
      "v_mfma_f32_16x16x32_bf16 v[36:39], v[248:251], v[220:223], v[36:39]\n\t"
      "v_bitop3_b32 v31, v31, v230, v231 bitop3:0x96\n\t"
      "s_waitcnt lgkmcnt(0)\n\t"
      "v_maximum3_f32 v252, v240, v241, v242\n\t"
      "v_maximum3_f32 v254, v243, v244, v245\n\t"
      "v_permlane16_swap_b32 v28, v29\n\t"
      "v_permlane16_swap_b32 v30, v31\n\t"
      "v_maximum3_f32 v252, v252, v246, v247\n\t"
      "s_mov_b64 vcc, %[odd]\n\t"
      "v_add_u32 v28, v28, v29\n\t"
      "v_xor_b32 v30, v30, v31\n\t"
      "v_maximum3_f32 v252, v252, v254, v254\n\t"
      "v_mov_b32 v29, v28\n\t"
      "v_mov_b32 v31, v30\n\t"
      "v_cndmask_b32 v253, v32, v36, vcc\n\t"
      "s_nop 0\n\t"
      "v_permlane32_swap_b32 v28, v29\n\t"
      "v_permlane32_swap_b32 v30, v31\n\t"
      "v_add_u32 v254, v252, %[tag]\n\t"
      "v_xor_b32 v255, v253, %[tag]\n\t"
      "v_add3_u32 v254, v254, v28, v29\n\t"
      "v_bitop3_b32 v255, v255, v30, v31 bitop3:0x69\n\t"
      "s_mov_b64 exec, %[lo32]\n\t"
      "buffer_store_dwordx4 v[252:255], %[fo], %[rs], 0 offen sc1\n\t"
      "s_mov_b64 exec, -1\n\t"
      "s_nop 1\n\t"
      :
      : "{v[64:95]}"(t0), "{v[96:127]}"(t1), "{v[128:159]}"(t2), "{v[160:191]}"(t3), [pr] "v"(pr), [mxr] "v"(mxr), [rs] "s"(rs), [ol] "v"(ol), [fo] "v"(fo), [tag] "v"(tag),
        [s0] "s"(0u), [s1] "s"(8192u), [s2] "s"(16384u), [s3] "s"(24576u), [lo8] "s"(0x00ff00ff00ff00ffull), [odd] "s"(0xffff0000ffff0000ull), [lo32] "s"(0xffffffffull)
      : "vcc", "scc", "memory", "v28", "v29", "v30", "v31", "v32", "v33", "v34", "v35", "v36", "v37", "v38", "v39", "v40", "v41", "v42", "v43", "v44", "v45", "v46", "v47", "v48", "v49", "v50", "v51", "v52", "v53", "v54", "v55", "v56", "v57", "v58", "v59", "v60", "v61", "v62", "v63", "v192", "v193", "v194", "v195", "v196", "v197", "v198", "v199", "v200", "v201", "v202", "v203", "v204", "v205", "v206", "v207", "v208", "v209", "v210", "v211", "v212", "v213", "v214", "v215", "v216", "v217", "v218", "v219", "v220", "v221", "v222", "v223", "v224", "v225", "v226", "v227", "v228", "v229", "v230", "v231", "v232", "v233", "v234", "v235", "v236", "v237", "v238", "v239", "v240", "v241", "v242", "v243", "v244", "v245", "v246", "v247", "v248", "v249", "v250", "v251", "v252", "v253", "v254", "v255");
}
DI void mlp2_publish(v4u af, const v32u& w0, const v32u& w1, const v32u& w2, rsrc_t rs, unsigned oq, int w) {
  asm volatile(
      "s_mov_b64 vcc, %[odd]\n\t"
      "s_waitcnt lgkmcnt(0)\n\t"
      "v_mfma_f32_16x16x32_bf16 v[160:163], v[60:63], v[64:67], 0\n\t"
      "v_mfma_f32_16x16x32_bf16 v[176:179], v[60:63], v[80:83], 0\n\t"
      "v_mfma_f32_16x16x32_bf16 v[164:167], v[60:63], v[68:71], 0\n\t"
      "v_mfma_f32_16x16x32_bf16 v[180:183], v[60:63], v[84:87], 0\n\t"
      "v_mfma_f32_16x16x32_bf16 v[168:171], v[60:63], v[72:75], 0\n\t"
      "v_mfma_f32_16x16x32_bf16 v[184:187], v[60:63], v[88:91], 0\n\t"
      "v_mfma_f32_16x16x32_bf16 v[172:175], v[60:63], v[76:79], 0\n\t"
      "v_mfma_f32_16x16x32_bf16 v[188:191], v[60:63], v[92:95], 0\n\t"
      "v_mfma_f32_16x16x32_bf16 v[192:195], v[60:63], v[96:99], 0\n\t"
      "v_mfma_f32_16x16x32_bf16 v[208:211], v[60:63], v[112:115], 0\n\t"
      "v_cndmask_b32 v56, v161, v177, vcc\n\t"
      "v_cndmask_b32 v57, v160, v176, vcc\n\t"
      "s_nop 0\n\t"
      "v_add_f32_dpp v40, v56, v57 row_ror:8 row_mask:0xf bank_mask:0xf\n\t"
      "v_min_u32 v40, -2, v40\n\t"
      "v_mfma_f32_16x16x32_bf16 v[196:199], v[60:63], v[100:103], 0\n\t"
      "v_cndmask_b32 v56, v165, v181, vcc\n\t"
      "v_cndmask_b32 v57, v164, v180, vcc\n\t"
      "v_mfma_f32_16x16x32_bf16 v[212:215], v[60:63], v[116:119], 0\n\t"
      "v_add_f32_dpp v41, v56, v57 row_ror:8 row_mask:0xf bank_mask:0xf\n\t"
      "v_min_u32 v41, -2, v41\n\t"
      "v_mfma_f32_16x16x32_bf16 v[200:203], v[60:63], v[104:107], 0\n\t"
      "v_cndmask_b32 v56, v169, v185, vcc\n\t"
      "v_cndmask_b32 v57, v168, v184, vcc\n\t"
      "v_mfma_f32_16x16x32_bf16 v[216:219], v[60:63], v[120:123], 0\n\t"
      "v_add_f32_dpp v42, v56, v57 row_ror:8 row_mask:0xf bank_mask:0xf\n\t"
      "v_min_u32 v42, -2, v42\n\t"
      "v_mfma_f32_16x16x32_bf16 v[204:207], v[60:63], v[108:111], 0\n\t"
      "v_cndmask_b32 v56, v173, v189, vcc\n\t"
      "v_cndmask_b32 v57, v172, v188, vcc\n\t"
      "v_mfma_f32_16x16x32_bf16 v[220:223], v[60:63], v[124:127], 0\n\t"
      "v_add_f32_dpp v43, v56, v57 row_ror:8 row_mask:0xf bank_mask:0xf\n\t"
      "v_min_u32 v43, -2, v43\n\t"
      "buffer_store_dwordx4 v[40:43], %[oq], %[rs], 0 offen offset:0 sc1\n\t"
      "s_cmp_lt_u32 %[w], 2\n\t"
      "s_cbranch_scc1 1f\n\t"
      "s_nop 1\n\t"
      "v_cndmask_b32 v56, v193, v209, vcc\n\t"
      "v_cndmask_b32 v57, v192, v208, vcc\n\t"
      "s_nop 0\n\t"
      "v_add_f32_dpp v44, v56, v57 row_ror:8 row_mask:0xf bank_mask:0xf\n\t"
      "v_min_u32 v44, -2, v44\n\t"
      "v_cndmask_b32 v56, v197, v213, vcc\n\t"
      "v_cndmask_b32 v57, v196, v212, vcc\n\t"
      "s_nop 0\n\t"
      "v_add_f32_dpp v45, v56, v57 row_ror:8 row_mask:0xf bank_mask:0xf\n\t"
      "v_min_u32 v45, -2, v45\n\t"
      "v_cndmask_b32 v56, v201, v217, vcc\n\t"
      "v_cndmask_b32 v57, v200, v216, vcc\n\t"
      "s_nop 0\n\t"
      "v_add_f32_dpp v46, v56, v57 row_ror:8 row_mask:0xf bank_mask:0xf\n\t"
      "v_min_u32 v46, -2, v46\n\t"
      "v_cndmask_b32 v56, v205, v221, vcc\n\t"
      "v_cndmask_b32 v57, v204, v220, vcc\n\t"
      "s_nop 0\n\t"
      "v_add_f32_dpp v47, v56, v57 row_ror:8 row_mask:0xf bank_mask:0xf\n\t"
      "v_min_u32 v47, -2, v47\n\t"
      "buffer_store_dwordx4 v[44:47], %[oq], %[rs], 0 offen offset:1024 sc1\n\t"
      "s_branch 2f\n\t"
      "1:\n\t"
      "v_mfma_f32_16x16x32_bf16 v[224:227], v[60:63], v[128:131], 0\n\t"
      "v_mfma_f32_16x16x32_bf16 v[240:243], v[60:63], v[144:147], 0\n\t"
      "v_cndmask_b32 v56, v193, v209, vcc\n\t"
      "v_cndmask_b32 v57, v192, v208, vcc\n\t"
      "s_nop 0\n\t"
      "v_add_f32_dpp v44, v56, v57 row_ror:8 row_mask:0xf bank_mask:0xf\n\t"
      "v_min_u32 v44, -2, v44\n\t"
      "v_mfma_f32_16x16x32_bf16 v[228:231], v[60:63], v[132:135], 0\n\t"
      "v_cndmask_b32 v56, v197, v213, vcc\n\t"
      "v_cndmask_b32 v57, v196, v212, vcc\n\t"
      "v_mfma_f32_16x16x32_bf16 v[244:247], v[60:63], v[148:151], 0\n\t"
      "v_add_f32_dpp v45, v56, v57 row_ror:8 row_mask:0xf bank_mask:0xf\n\t"
      "v_min_u32 v45, -2, v45\n\t"
      "v_mfma_f32_16x16x32_bf16 v[232:235], v[60:63], v[136:139], 0\n\t"
      "v_cndmask_b32 v56, v201, v217, vcc\n\t"
      "v_cndmask_b32 v57, v200, v216, vcc\n\t"
      "v_mfma_f32_16x16x32_bf16 v[248:251], v[60:63], v[152:155], 0\n\t"
      "v_add_f32_dpp v46, v56, v57 row_ror:8 row_mask:0xf bank_mask:0xf\n\t"
      "v_min_u32 v46, -2, v46\n\t"
      "v_mfma_f32_16x16x32_bf16 v[236:239], v[60:63], v[140:143], 0\n\t"
      "v_cndmask_b32 v56, v205, v221, vcc\n\t"
      "v_cndmask_b32 v57, v204, v220, vcc\n\t"
      "v_mfma_f32_16x16x32_bf16 v[252:255], v[60:63], v[156:159], 0\n\t"
      "v_add_f32_dpp v47, v56, v57 row_ror:8 row_mask:0xf bank_mask:0xf\n\t"
      "v_min_u32 v47, -2, v47\n\t"
      "buffer_store_dwordx4 v[44:47], %[oq], %[rs], 0 offen offset:1024 sc1\n\t"
      "s_nop 1\n\t"
      "v_cndmask_b32 v56, v225, v241, vcc\n\t"
      "v_cndmask_b32 v57, v224, v240, vcc\n\t"
      "s_nop 0\n\t"
      "v_add_f32_dpp v48, v56, v57 row_ror:8 row_mask:0xf bank_mask:0xf\n\t"
      "v_min_u32 v48, -2, v48\n\t"
      "v_cndmask_b32 v56, v229, v245, vcc\n\t"
      "v_cndmask_b32 v57, v228, v244, vcc\n\t"
      "s_nop 0\n\t"
      "v_add_f32_dpp v49, v56, v57 row_ror:8 row_mask:0xf bank_mask:0xf\n\t"
      "v_min_u32 v49, -2, v49\n\t"
      "v_cndmask_b32 v56, v233, v249, vcc\n\t"
      "v_cndmask_b32 v57, v232, v248, vcc\n\t"
      "s_nop 0\n\t"
      "v_add_f32_dpp v50, v56, v57 row_ror:8 row_mask:0xf bank_mask:0xf\n\t"
      "v_min_u32 v50, -2, v50\n\t"
      "v_cndmask_b32 v56, v237, v253, vcc\n\t"
      "v_cndmask_b32 v57, v236, v252, vcc\n\t"
      "s_nop 0\n\t"
      "v_add_f32_dpp v51, v56, v57 row_ror:8 row_mask:0xf bank_mask:0xf\n\t"
      "v_min_u32 v51, -2, v51\n\t"
      "buffer_store_dwordx4 v[48:51], %[oq], %[rs], 0 offen offset:2048 sc1\n\t"
      "2:\n\t"
      "s_nop 1\n\t"
      :
      : "{v[60:63]}"(af), "{v[64:95]}"(w0), "{v[96:127]}"(w1), "{v[128:159]}"(w2), [rs] "s"(rs), [oq] "v"(oq), [w] "s"(w), [odd] "s"(0xffff0000ffff0000ull)
      : "vcc", "scc", "memory", "v40", "v41", "v42", "v43", "v44", "v45", "v46", "v47", "v48", "v49", "v50", "v51", "v52", "v53", "v54", "v55", "v56", "v57", "v160", "v161", "v162", "v163", "v164", "v165", "v166", "v167", "v168", "v169", "v170", "v171", "v172", "v173", "v174", "v175", "v176", "v177", "v178", "v179", "v180", "v181", "v182", "v183", "v184", "v185", "v186", "v187", "v188", "v189", "v190", "v191", "v192", "v193", "v194", "v195", "v196", "v197", "v198", "v199", "v200", "v201", "v202", "v203", "v204", "v205", "v206", "v207", "v208", "v209", "v210", "v211", "v212", "v213", "v214", "v215", "v216", "v217", "v218", "v219", "v220", "v221", "v222", "v223", "v224", "v225", "v226", "v227", "v228", "v229", "v230", "v231", "v232", "v233", "v234", "v235", "v236", "v237", "v238", "v239", "v240", "v241", "v242", "v243", "v244", "v245", "v246", "v247", "v248", "v249", "v250", "v251", "v252", "v253", "v254", "v255");
}
DI void mlp1_partial(unsigned h0, unsigned h1, const v32u& wa, const v32u& wb, const v16u& wc, float act, LAS float* stw, LAS char* dw, unsigned cnt) {
  asm volatile(
      "s_nop 1\n\t"
      "v_mov_b32_dpp v34, v32 row_shl:1 row_mask:0xf bank_mask:0xf bound_ctrl:1\n\t"
      "v_mov_b32_dpp v35, v33 row_shl:1 row_mask:0xf bank_mask:0xf bound_ctrl:1\n\t"
      "v_mov_b32_dpp v36, v32 row_shl:2 row_mask:0xf bank_mask:0xf bound_ctrl:1\n\t"
      "v_mov_b32_dpp v37, v33 row_shl:2 row_mask:0xf bank_mask:0xf bound_ctrl:1\n\t"
      "v_mfma_f32_16x16x32_bf16 v[144:147], v[64:67], v[32:35], 0\n\t"
      "v_mov_b32_dpp v38, v32 row_shl:3 row_mask:0xf bank_mask:0xf bound_ctrl:1\n\t"
      "v_mov_b32_dpp v39, v33 row_shl:3 row_mask:0xf bank_mask:0xf bound_ctrl:1\n\t"
      "v_lshlrev_b32 v52, 16, v32\n\t"
      "v_mfma_f32_16x16x32_bf16 v[148:151], v[68:71], v[32:35], 0\n\t"
      "v_and_b32 v53, 0xffff0000, v32\n\t"
      "v_lshlrev_b32 v54, 16, v33\n\t"
      "v_and_b32 v55, 0xffff0000, v33\n\t"
      "v_mfma_f32_16x16x32_bf16 v[152:155], v[72:75], v[32:35], 0\n\t"
      "v_mov_b32_dpp v40, v32 row_shl:4 row_mask:0xf bank_mask:0xf bound_ctrl:1\n\t"
      "v_mov_b32_dpp v41, v33 row_shl:4 row_mask:0xf bank_mask:0xf bound_ctrl:1\n\t"
      "v_mov_b32_dpp v42, v32 row_shl:5 row_mask:0xf bank_mask:0xf bound_ctrl:1\n\t"
      "v_mfma_f32_16x16x32_bf16 v[156:159], v[76:79], v[32:35], 0\n\t"
      "v_mov_b32_dpp v43, v33 row_shl:5 row_mask:0xf bank_mask:0xf bound_ctrl:1\n\t"
      "v_add_f32 v56, v52, v53\n\t"
      "v_mul_f32 v57, v52, v52\n\t"
      "v_mfma_f32_16x16x32_bf16 v[144:147], v[80:83], v[36:39], v[144:147]\n\t"
      "v_add_f32 v58, v54, v55\n\t"
      "v_fmac_f32 v57, v53, v53\n\t"
      "v_add_f32 v56, v56, v58\n\t"
      "v_mfma_f32_16x16x32_bf16 v[148:151], v[84:87], v[36:39], v[148:151]\n\t"
      "v_fmac_f32 v57, v54, v54\n\t"
      "v_mul_f32 v56, v56, %[act]\n\t"
      "v_fmac_f32 v57, v55, v55\n\t"
      "v_mfma_f32_16x16x32_bf16 v[152:155], v[88:91], v[36:39], v[152:155]\n\t"
      "s_nop 0\n\t"
      "v_mul_f32 v57, v57, %[act]\n\t"
      "v_mov_b32_dpp v44, v32 row_shl:6 row_mask:0xf bank_mask:0xf bound_ctrl:1\n\t"
      "v_mfma_f32_16x16x32_bf16 v[156:159], v[92:95], v[36:39], v[156:159]\n\t"
      "v_mov_b32_dpp v45, v33 row_shl:6 row_mask:0xf bank_mask:0xf bound_ctrl:1\n\t"
      "v_mov_b32_dpp v46, v32 row_shl:7 row_mask:0xf bank_mask:0xf bound_ctrl:1\n\t"
      "v_mov_b32_dpp v47, v33 row_shl:7 row_mask:0xf bank_mask:0xf bound_ctrl:1\n\t"
      "v_mfma_f32_16x16x32_bf16 v[144:147], v[96:99], v[40:43], v[144:147]\n\t"
      "v_add_f32_dpp v56, v56, v56 row_ror:8 row_mask:0xf bank_mask:0xf\n\t"
      "s_nop 0\n\t"
      "v_add_f32_dpp v57, v57, v57 row_ror:8 row_mask:0xf bank_mask:0xf\n\t"
      "v_mfma_f32_16x16x32_bf16 v[148:151], v[100:103], v[40:43], v[148:151]\n\t"
      "v_add_f32_dpp v56, v56, v56 row_ror:4 row_mask:0xf bank_mask:0xf\n\t"
      "s_nop 0\n\t"
      "v_add_f32_dpp v57, v57, v57 row_ror:4 row_mask:0xf bank_mask:0xf\n\t"
      "v_mfma_f32_16x16x32_bf16 v[152:155], v[104:107], v[40:43], v[152:155]\n\t"
      "v_mov_b32_dpp v48, v32 row_shl:8 row_mask:0xf bank_mask:0xf bound_ctrl:1\n\t"
      "v_mov_b32_dpp v49, v33 row_shl:8 row_mask:0xf bank_mask:0xf bound_ctrl:1\n\t"
      "v_mov_b32_dpp v50, v32 row_shl:9 row_mask:0xf bank_mask:0xf bound_ctrl:1\n\t"
      "v_mfma_f32_16x16x32_bf16 v[156:159], v[108:111], v[40:43], v[156:159]\n\t"
      "v_mov_b32_dpp v51, v33 row_shl:9 row_mask:0xf bank_mask:0xf bound_ctrl:1\n\t"
      "v_add_f32_dpp v56, v56, v56 row_ror:2 row_mask:0xf bank_mask:0xf\n\t"
      "s_nop 0\n\t"
      "v_mfma_f32_16x16x32_bf16 v[144:147], v[112:115], v[44:47], v[144:147]\n\t"
      "v_add_f32_dpp v57, v57, v57 row_ror:2 row_mask:0xf bank_mask:0xf\n\t"
      "v_add_f32_dpp v56, v56, v56 row_ror:1 row_mask:0xf bank_mask:0xf\n\t"
      "s_nop 0\n\t"
      "v_mfma_f32_16x16x32_bf16 v[148:151], v[116:119], v[44:47], v[148:151]\n\t"
      "v_add_f32_dpp v57, v57, v57 row_ror:1 row_mask:0xf bank_mask:0xf\n\t"
      "s_nop 0\n\t"
      "v_mfma_f32_16x16x32_bf16 v[152:155], v[120:123], v[44:47], v[152:155]\n\t"
      "v_permlane16_swap_b32 v56, v57\n\t"
      "v_add_f32 v56, v56, v57\n\t"
      "v_mov_b32 v57, v56\n\t"
      "v_mfma_f32_16x16x32_bf16 v[156:159], v[124:127], v[44:47], v[156:159]\n\t"
      "s_nop 0\n\t"
      "v_mfma_f32_16x16x32_bf16 v[144:147], v[128:131], v[48:51], v[144:147]\n\t"
      "v_permlane32_swap_b32 v56, v57\n\t"
      "v_add_f32 v56, v56, v57\n\t"
      "ds_write_b32 %[stw], v56\n\t"
      "v_mfma_f32_16x16x32_bf16 v[148:151], v[132:135], v[48:51], v[148:151]\n\t"
      "s_mov_b64 exec, 1\n\t"
      "ds_add_u32 %[cnt], %[one]\n\t"
      "s_mov_b64 exec, -1\n\t"
      "v_mfma_f32_16x16x32_bf16 v[152:155], v[136:139], v[48:51], v[152:155]\n\t"
      "v_mfma_f32_16x16x32_bf16 v[156:159], v[140:143], v[48:51], v[156:159]\n\t"
      "s_mov_b64 exec, %[col0]\n\t"
      "s_nop 3\n\t"
      "ds_write_b128 %[dw], v[144:147] offset:0\n\t"
      "ds_write_b128 %[dw], v[148:151] offset:64\n\t"
      "ds_write_b128 %[dw], v[152:155] offset:128\n\t"
      "s_nop 0\n\t"
      "ds_write_b128 %[dw], v[156:159] offset:192\n\t"
      "s_mov_b64 exec, -1\n\t"
      "s_waitcnt lgkmcnt(0)\n\t"
      "s_barrier\n\t"
      :
      : "{v32}"(h0), "{v33}"(h1), "{v[64:95]}"(wa), "{v[96:127]}"(wb), "{v[128:143]}"(wc), [act] "v"(act), [stw] "v"(stw), [dw] "v"(dw), [cnt] "v"(cnt), [one] "v"(1u),
        [col0] "s"(0x0001000100010001ull)
      : "memory", "v34", "v35", "v36", "v37", "v38", "v39", "v40", "v41", "v42", "v43", "v44", "v45", "v46", "v47", "v48", "v49", "v50", "v51", "v52", "v53", "v54", "v55", "v56", "v57", "v58", "v59", "v144", "v145", "v146", "v147", "v148", "v149", "v150", "v151", "v152", "v153", "v154", "v155", "v156", "v157", "v158", "v159");
}

DI unsigned now32() { return (unsigned)__builtin_amdgcn_s_memrealtime(); }
DI void wait_until(unsigned t) {
  unsigned c, n;
  asm volatile("s_mov_b32 %1, 600\n\t1:\n\ts_memrealtime vcc\n\ts_waitcnt lgkmcnt(0)\n\ts_sub_u32 %0, vcc_lo, %2\n\ts_cmp_ge_i32 %0, 0\n\ts_cbranch_scc1 2f\n\ts_cmp_ge_i32 %0, -8\n\ts_cbranch_scc1 3f\n\ts_sleep 2\n\t3:\n\ts_sub_u32 %1, %1, 1\n\ts_cbranch_scc0 1b\n\t2:"
               : "=&s"(c), "=&s"(n) : "s"(t) : "vcc", "scc", "memory");
}
DI void hint_max(const v4u (&hv)[2], unsigned base, int (&m)[2]) {
#pragma unroll
  for (int i = 0; i < 2; ++i) {
    int d = (int)(hv[i].x - base);
    d = max(d, (int)dppu<0x128>(d)); d = max(d, (int)dppu<0x124>(d)); d = max(d, (int)dppu<0x122>(d)); d = max(d, (int)dppu<0x121>(d));
    const auto r = __builtin_amdgcn_permlane16_swap((unsigned)d, (unsigned)d, false, false);
    m[i] = max((int)r[0], (int)r[1]);
  }
}
DI void wait_ck_or(LAS unsigned* p, unsigned t) {
  unsigned c, n, v;
  asm volatile("s_mov_b32 %1, 0x1000\n\t1:\n\tds_read_b32 %2, %3\n\ts_memrealtime vcc\n\ts_waitcnt lgkmcnt(0)\n\tv_readfirstlane_b32 %0, %2\n\ts_cmp_ge_u32 %0, 2\n\ts_cbranch_scc1 2f\n\t"
               "s_sub_u32 %0, vcc_lo, %4\n\ts_cmp_ge_i32 %0, 0\n\ts_cbranch_scc1 2f\n\ts_sleep 1\n\ts_sub_u32 %1, %1, 1\n\ts_cbranch_scc0 1b\n\t2:"
               : "=&s"(c), "=&s"(n), "=&v"(v) : "v"(lds_addr(p)), "s"(t) : "vcc", "scc", "memory");
}
DI unsigned latest4(LAS unsigned* p) {
  const v4u t = *(LAS v4u*)p;
  const int m = max(max(0, (int)(t.y - t.x)), max((int)(t.z - t.x), (int)(t.w - t.x)));
  return __builtin_amdgcn_readfirstlane(t.x + (unsigned)m);
}
template <class T> DI T opaque(T p) { asm volatile("" : "+v"(p)); return p; }
#define PIN4(a, b, c, d) asm volatile("" : "+v"(a), "+v"(b), "+v"(c), "+v"(d))

__global__ void __launch_bounds__(512, 2) __attribute__((amdgpu_waves_per_eu(2, 2)))
mla_block(const u16* __restrict__ wq, const u16* __restrict__ x, unsigned char* ws, const u16* __restrict__ wuk, const u16* __restrict__ ckv,
          const u16* __restrict__ wuv, const u16* __restrict__ wo, const u16* __restrict__ w1, const u16* __restrict__ w2, u16* __restrict__ out) {
  extern __shared__ __attribute__((aligned(16))) char smem_[];
  LAS char* L = (LAS char*)smem_;
  LAS unsigned* ctl = (LAS unsigned*)(L + L_CTL);
  const unsigned lbase = (unsigned)(size_t)L;
  const unsigned tstart = now32();
  const int wave = __builtin_amdgcn_readfirstlane(threadIdx.x >> 6);
#define LANE_CONSTS() \
  int lane; asm volatile("v_mbcnt_lo_u32_b32 %0, -1, 0\n\tv_mbcnt_hi_u32_b32 %0, -1, %0" : "=v"(lane)); \
  const int g = lane >> 4, n = lane & 15, r8 = lane >> 3, c8 = lane & 7, q4 = n >> 2, p4 = n & 3; \
  const unsigned sw128 = (unsigned)((c8 ^ (((r8 >> 1) & 3) << 1)) * 16);     \
  const unsigned rd128 = (unsigned)((4 * g + q4) * 128 + (p4 & 1) * 8);      \
  const int xr128 = ((((4 * g + q4) >> 1) & 3) << 1) ^ (p4 >> 1); \
  (void)sw128; (void)rd128; (void)xr128; (void)r8; (void)c8
  const int blk = blockIdx.x, xcd = blk & 7, r = blk >> 3;
  if (wave == 7) {
    unsigned long long pc; unsigned len, l2;
    asm volatile("s_getpc_b64 %0\n.Lmla_code_here:\n\ts_mov_b32 %1, .Lmla_end_a-.Lmla_code_here\n\ts_mov_b32 %2, .Lmla_end_b-.Lmla_code_here\n\ts_max_i32 %1, %1, %2\n\t"
                 "s_mov_b32 %2, .Lmla_end_c-.Lmla_code_here\n\ts_max_i32 %1, %1, %2" : "=s"(pc), "=&s"(len), "=&s"(l2)::"scc");
    const unsigned t = threadIdx.x & 63;
    asm volatile("s_mov_b32 m0, %0\n\ts_nop 0\n\tbuffer_load_dword %1, %2, 0 offen lds" ::"s"(lbase + L_SINK), "v"(t < 12 ? (t * 32 + (unsigned)r) * 128u : 0x40000000u), "s"(mk_rsrc((const void*)pc, len)) : "memory");
  }
  asm volatile("" ::"s"(x), "s"(ckv), "s"(wq), "s"(wuk), "s"(wuv), "s"(wo), "s"(w1), "s"(w2), "s"(out), "s"(ws));
  const rsrc_t rws = mk_rsrc(ws, WS_END);
  unsigned tag = 0;
#define GET_TAG(SLEEP) do { lds_wait_ge<SLEEP>(ctl + C_XN, 1u); tag = *(LAS unsigned*)(L + L_ST + 32); } while (0)
  const int isK = (r >> KBIT) & 1, rr = ((r >> (KBIT + 1)) << KBIT) | (r & ((1 << KBIT) - 1));
  const int h = xcd * 4 + (rr >> 2), kq = rr & 3;
  const int chunk = xcd * 16 + rr;
  const bool isT2 = rr < 10;
  const int jr = xcd + 8 * (rr >> 2), b3 = rr & 3;
  const bool isT3 = jr < 27;
  const rsrc_t rx = mk_rsrc(x, NB * MD * 2);

  if (threadIdx.x >= 448 && threadIdx.x < 496) lds_st(ctl + (threadIdx.x - 448), 0u);

  {
    LANE_CONSTS();
    v4u xr[5];
    unsigned epoch;
    if (wave == 0) {
      __builtin_amdgcn_s_setprio(2);
      const rsrc_t rq = mk_rsrc(wq, MD * NH * HD * 2);
      const unsigned vo = (unsigned)(160 * kq + r8) * 8192u + (unsigned)(h * 256 + isK * 128) + sw128;
      dma_run<20, 1024, 65536>(lbase + L_WQ, vo, rq, 0u);
    } else if (wave == 1) {
      __builtin_amdgcn_s_setprio(2);
#pragma unroll
      for (int i = 0; i < 5; ++i) asm volatile("buffer_load_dwordx4 %0, %1, %2, 0 offen offset:%3" : "=v"(xr[i]) : "v"((unsigned)(g * 1280 + n * 80)), "s"(rx), "i"(i * 16) : "memory");
      asm volatile("buffer_load_dword %0, off, %1, 0 sc1" : "=v"(epoch) : "s"(rws) : "memory");
    } else if (wave == 3 && !isK) {
      const v4u blank = {BLANK, BLANK, BLANK, BLANK};
      st_sc1(rws, WS_QA + (unsigned)((g * 32 + h) * 1024 + kq * 256 + n * 16), blank);
      st_sc1(rws, WS_CX + (unsigned)((h * 4 + kq) * 1024 + lane * 16), blank);
      if (isT2) st_sc1(rws, WS_HP + (unsigned)((g * 8 + xcd) * 2560 + rr * 256 + n * 16), blank);
    }
    bar();
    if (!isK) {
      const rsrc_t ruk = mk_rsrc(wuk, DC * NH * HD * 2);
      const unsigned vo = (unsigned)(128 * kq + 16 * wave + g) * 8192u + (unsigned)(h * 256 + (n ^ g) * 16);
#pragma unroll
      for (int q = 0; q < 4; ++q) dma16(lbase + L_UK + wave * 4096 + q * 1024, vo ^ (unsigned)(q * 64), ruk, (unsigned)q * 32768u);
      if (wave == 3) { vm_wait<4>(); GET_TAG(1); if (lane == 0) st_sc1(rws, WS_EV + (unsigned)(chunk * 16), v4u{0u, tag, 0u, tag}); }
    }
    if (wave == 1) {
      if (isK) vm_wait<1>(); else vm_wait<5>();
      asm volatile("" : "+v"(xr[0]), "+v"(xr[1]), "+v"(xr[2]), "+v"(xr[3]), "+v"(xr[4])::"memory");
#pragma unroll
      for (int i = 0; i < 5; ++i) *(LAS v4u*)(L + L_XS + g * 1296 + n * 80 + i * 16) = xr[i];
      lgkm0();
      lds_st(ctl + C_XR, 1u);
      f32x2 sa = {0.f, 0.f}, sb = {0.f, 0.f};
#pragma unroll
      for (int i = 0; i < 5; ++i)
#pragma unroll
        for (int k = 0; k < 4; k += 2) { sa += f32x2{lo16(xr[i][k]), hi16(xr[i][k])}; sb += f32x2{lo16(xr[i][k + 1]), hi16(xr[i][k + 1])}; }
      const float mean = row_sum((sa[0] + sa[1]) + (sb[0] + sb[1])) * (1.f / MD);
      const f32x2 m2 = {mean, mean};
      f32x2 qa = {0.f, 0.f}, qb = {0.f, 0.f};
#pragma unroll
      for (int i = 0; i < 5; ++i)
#pragma unroll
        for (int k = 0; k < 4; k += 2) {
          const f32x2 d0 = f32x2{lo16(xr[i][k]), hi16(xr[i][k])} - m2, d1 = f32x2{lo16(xr[i][k + 1]), hi16(xr[i][k + 1])} - m2;
          qa += d0 * d0; qb += d1 * d1;
        }
      const float var = row_sum((qa[0] + qa[1]) + (qb[0] + qb[1])) * (1.f / MD);
      if (n == 0) *(LAS f32x2*)(L + L_ST + g * 8) = f32x2{rbf(mean), rcp(rbf(__builtin_amdgcn_sqrtf(rbf(rbf(var) + 1e-5f))))};
      if (isK) vm_wait<0>(); else vm_wait<4>();
      asm volatile("v_add_u32 %0, 1, %1" : "=v"(tag) : "v"(epoch));
      *(LAS unsigned*)(L + L_ST + 32) = tag;
      lgkm0();
      lds_st(ctl + C_XN, 1u);
    }
    if (wave == 0) {
      f32x4 acc[4], cs[4];
#pragma unroll
      for (int t = 0; t < 4; ++t) { acc[t] = f32x4{0.f, 0.f, 0.f, 0.f}; cs[t] = f32x4{0.f, 0.f, 0.f, 0.f}; }
      LAS char* wp = L + L_WQ + rd128;
      const v4u ones = {0x3f803f80u, 0x3f803f80u, 0x3f803f80u, 0x3f803f80u};
      const unsigned qxo = WS_QX + (unsigned)(((h * 16 + kq * 4 + p4) * 32 + isK * 16 + q4 * 4 + g) * 16);
      lds_wait_ge(ctl + C_XR, 1u);
      v4u xf[5];
      LAS char* xp = L + L_XS + p4 * 1296 + kq * 320 + g * 8;
#pragma unroll
      for (int s = 0; s < 5; ++s) { const v2u a = *(LAS v2u*)(xp + s * 64), c = *(LAS v2u*)(xp + s * 64 + 32); xf[s] = v4u{a.x, a.y, c.x, c.y}; }
#define P1_STEP(S) do { if (isK) vm_wait<16 - 4 * (S)>(); else vm_wait<20 - 4 * (S)>(); v4u wf[4]; \
        _Pragma("unroll") for (int t = 0; t < 4; ++t) wf[t] = tr16(wp + (S) * 4096 + (((2 * t) ^ xr128) * 16), 2048); \
        fence_sched(); \
        _Pragma("unroll") for (int t = 0; t < 4; ++t) { acc[t] = mfma(wf[t], xf[S], acc[t]); cs[t] = mfma(wf[t], ones, cs[t]); } } while (0)
      P1_STEP(0); P1_STEP(1); P1_STEP(2); P1_STEP(3);
      GET_TAG(0);
      const f32x2 st = *(LAS f32x2*)(L + L_ST + p4 * 8);
      P1_STEP(4);
#undef P1_STEP
      const bool lo8 = n < 8, odd = q4 & 1;
      float qv[4];
#pragma unroll
      for (int i = 0; i < 4; ++i) {
        const float a = sel(lo8, sel(odd, acc[1][i], acc[0][i]), sel(odd, acc[3][i], acc[2][i])), c = sel(lo8, sel(odd, cs[1][i], cs[0][i]), sel(odd, cs[3][i], cs[2][i]));
        qv[i] = (a - st[0] * c) * st[1];
      }
      st_sc1(rws, qxo, v4u{pk(qv[0], qv[1]), tag, pk(qv[2], qv[3]), tag});
      lds_st(ctl + C_QP, 1u);
    }
  }

  if (!isK) {
    {
      LANE_CONSTS();
      if (wave == 0 || wave == 3) vm_wait<1>(); else vm_wait<0>();
      if (wave >= 2) GET_TAG(1);
      v4u uk[4];
#pragma unroll
      for (int s = 0; s < 4; ++s) uk[s] = *(LAS v4u*)(L + L_UK + wave * 4096 + n * 256 + (((4 * s + g) ^ n) * 16));
      v4u u[1];
      lds_wait_ge<1>(ctl + C_QP, 1u);
      wait_until(now32() + H1_D);
      poll_tag1q<0>(rws, WS_QX + (unsigned)(h * 8192 + wave * 1024 + lane * 16), tag, u);
      *(LAS v2u*)(L + L_QG + (wave * 2 + (lane >> 5)) * 272 + (lane & 31) * 8) = v2u{u[0].x, u[0].z};
      bar();
      v4u qv[4];
#pragma unroll
      for (int s = 0; s < 4; ++s) qv[s] = *(LAS v4u*)(L + L_QG + n * 272 + s * 64 + g * 16);
      fence_sched();
      f32x4 acc = {0.f, 0.f, 0.f, 0.f};
#pragma unroll
      for (int s = 0; s < 4; ++s) acc = mfma(uk[s], qv[s], acc);
#pragma unroll
      for (int i = 0; i < 4; ++i) { acc[i] += dpp<0x128>(acc[i]); acc[i] += dpp<0x124>(acc[i]); }
      constexpr float SC = 0.08838834764831845f * 1.4426950408889634f;
      const v2u qo = {nb(pk(acc[0] * SC, acc[1] * SC)), nb(pk(acc[2] * SC, acc[3] * SC))};
      if (n < 4) *(LAS v2u*)(L + L_QO + n * 256 + wave * 32 + g * 8) = qo;
      if (last_of<8>(ctl + C_QN)) st_sc1(rws, WS_QA + (unsigned)((g * 32 + h) * 1024 + kq * 256 + n * 16), *(LAS v4u*)(L + L_QO + lane * 16));
    }
    if (wave < 4) {
      const int w = wave;
      LANE_CONSTS();
      __builtin_amdgcn_s_setprio(0);
      wait_until(tstart + QT1_AT);
      const rsrc_t ruv = mk_rsrc(wuv, DC * NH * HD * 2);
      const unsigned rowq = (unsigned)(128 * kq + 32 * w + g);
      const unsigned ve = rowq * 8192u + (unsigned)(h * 256) + (unsigned)((n ^ (g << 1)) * 16);
      const unsigned vo = rowq * 8192u + (unsigned)(h * 256) + (unsigned)((n ^ ((4 + g) << 1)) * 16);
      const rsrc_t ro = mk_rsrc(wo, NH * HD * MD * 2);
      const unsigned v2 = (unsigned)(512 * xcd + 128 * w + r8) * 1280u + (unsigned)(rr * 128) + sw128;
#pragma unroll 1
      for (unsigned q = 0; q < 8; q += 2) {
        dma16(lbase + L_T1W + w * 8192 + q * 1024, ve, ruv, q * 32768u);
        dma16(lbase + L_T1W + w * 8192 + q * 1024 + 1024, vo, ruv, q * 32768u + 32768u);
      }
      if (isT2) {
        dma_run<2, 1024, 10240>(lbase + L_T2W + w * 16384, v2, ro, 0u);
        vm_wait<2>();
        lds_add1(ctl + C_W1);
        dma_run<14, 1024, 10240>(lbase + L_T2W + w * 16384 + 2048, v2, ro, 20480u);
        vm_wait<0>();
      } else {
        vm_wait<0>();
        lds_add1(ctl + C_W1);
      }
    }
    const int w = wave & 3, pp = wave >> 2;
    if (pp) {
      LANE_CONSTS();
      lds_wait_ge<1>(ctl + C_W1, 4u);
      v4u wf[4][8];
      {
        const int row = 4 * g + q4;
        LAS char* wp = L + L_T1W + row * 256 + (p4 & 1) * 8;
#pragma unroll
        for (int kk = 0; kk < 4; ++kk)
#pragma unroll
          for (int t = 0; t < 8; ++t) wf[kk][t] = tr16(wp + kk * 8192 + (((2 * t + (p4 >> 1)) ^ ((row & 7) << 1)) * 16), 4096);
      }
      lgkm0();
      unsigned tb;
      {
        v4u hv[1];
        wait_until(tstart + RES_AT);
        poll_tag1h<0>(rws, WS_H1 + (unsigned)((xcd * 128 + w * 32 + (lane & 31)) * 128), tag, hv);
        const unsigned base = __builtin_amdgcn_readfirstlane(hv[0].x);
        int d = (int)(hv[0].x - base);
        d = max(d, (int)dppu<0x128>(d)); d = max(d, (int)dppu<0x124>(d)); d = max(d, (int)dppu<0x122>(d)); d = max(d, (int)dppu<0x121>(d));
        const auto r2 = __builtin_amdgcn_permlane16_swap((unsigned)d, (unsigned)d, false, false);
        tb = __builtin_amdgcn_readfirstlane(base + (unsigned)max((int)r2[0], (int)r2[1]));
        lds_st(ctl + C_TF + w, tb);
      }
      const unsigned ob = opaque(WS_OP + (unsigned)((((w * 32 + (n & 7)) * 32 + h) * 64 + (4 * kq + (n >> 3)) * 4 + g) * 16));
      const unsigned co = opaque(WS_CX + (unsigned)(((h * 4 + kq) * 4 + w) * 256 + (g * 4 + q4) * 16));
      const unsigned fo = opaque(FL_OFF(h, kq, w * 32 + 8 * g + (n & 7)));
      wait_until(tb);
      f32x4 acc[8];
      float inv;
#pragma unroll 1
      for (unsigned tries = 0; tries < SPIN_CAP; ++tries) {
        v4u o[2][4];
        const v4u fu = ld_sc1(rws, fo);
        fence_sched();
#pragma unroll
        for (int k = 0; k < 2; ++k)
#pragma unroll
          for (int i = 0; i < 4; ++i) { o[k][i] = __builtin_amdgcn_raw_buffer_load_b128(rws, ob + k * 128, i * 8 * 32768, AUX_SC1); fence_sched(); }
        const float ms = __uint_as_float(fu.x), lsum = __uint_as_float(fu.y);
        const float e = ex2(ms - x32_max(x16_max(row_max8(ms))));
        const auto r1 = __builtin_amdgcn_permlane16_swap(__float_as_uint(e), __float_as_uint(e), false, false);
        const auto r2 = __builtin_amdgcn_permlane32_swap(r1[0], r1[0], false, false), r3 = __builtin_amdgcn_permlane32_swap(r1[1], r1[1], false, false);
        const float al[4] = {__uint_as_float(r2[0]), __uint_as_float(r3[0]), __uint_as_float(r2[1]), __uint_as_float(r3[1])};
        const bool lo8 = n < 8;
        v4u ae[2], ao[2];
#define T1_COMBINE(k) _Pragma("unroll") for (int j = 0; j < 4; ++j) { \
          f32x2 t = f32x2{lo16(o[k][0][j]), hi16(o[k][0][j])} * al[0]; \
          _Pragma("unroll") for (int i = 1; i < 4; ++i) t = __builtin_elementwise_fma(f32x2{lo16(o[k][i][j]), hi16(o[k][i][j])}, f32x2{al[i], al[i]}, t); \
          const unsigned af = pk(t[0], t[1]); \
          ae[k][j] = lo8 ? af : 0u; ao[k][j] = lo8 ? 0u : af; }
        T1_COMBINE(0)
        fence_sched();
#pragma unroll
        for (int t = 0; t < 8; ++t) acc[t] = mfma(ae[0], wf[0][t], f32x4{0.f, 0.f, 0.f, 0.f});
#pragma unroll
        for (int t = 0; t < 8; ++t) acc[t] = mfma(ao[0], wf[1][t], acc[t]);
        T1_COMBINE(1)
#undef T1_COMBINE
#pragma unroll
        for (int k = 0; k < 16; ++k) { __builtin_amdgcn_sched_group_barrier(0x8, 1, 0); __builtin_amdgcn_sched_group_barrier(0x2, 4, 0); }
        fence_sched();
#pragma unroll
        for (int t = 0; t < 8; ++t) acc[t] = mfma(ae[1], wf[2][t], acc[t]);
#pragma unroll
        for (int t = 0; t < 8; ++t) acc[t] = mfma(ao[1], wf[3][t], acc[t]);
        unsigned cs[4], cx[4];
#pragma unroll
        for (int i = 0; i < 4; ++i) {
          cs[i] = add3(add3(add3(o[0][i][0], o[0][i][1], o[0][i][2]), o[0][i][3], o[1][i][0]), o[1][i][1], o[1][i][2]) + o[1][i][3];
          cx[i] = xor3(xor3(xor3(o[0][i][0], o[0][i][1], o[0][i][2]), o[0][i][3], o[1][i][0]), o[1][i][1], o[1][i][2]) ^ o[1][i][3];
        }
        const auto a0 = __builtin_amdgcn_permlane32_swap(cs[0], cs[2], false, false), a1 = __builtin_amdgcn_permlane32_swap(cs[1], cs[3], false, false);
        const auto x0 = __builtin_amdgcn_permlane32_swap(cx[0], cx[2], false, false), x1 = __builtin_amdgcn_permlane32_swap(cx[1], cx[3], false, false);
        const auto a2 = __builtin_amdgcn_permlane16_swap(a0[0] + a0[1], a1[0] + a1[1], false, false), x2 = __builtin_amdgcn_permlane16_swap(x0[0] ^ x0[1], x1[0] ^ x1[1], false, false);
        const unsigned sa = a2[0] + a2[1], sx = x2[0] ^ x2[1];
        const bool bad = (sa + dppu<0x128>(sa) + fu.x + tag != fu.z) | (~(sx ^ dppu<0x128>(sx) ^ fu.y ^ tag) != fu.w);
        inv = rcp(x32_sum(x16_sum(row_sum8(e * lsum))));
#pragma unroll
        for (int k = 0; k < 16; ++k) { __builtin_amdgcn_sched_group_barrier(0x8, 1, 1); __builtin_amdgcn_sched_group_barrier(0x2, 4, 1); }
        fence_sched();
        if (!__any(bad)) break;
        __builtin_amdgcn_s_sleep(T1_SLEEP);
        asm volatile("" ::: "memory");
      }
      const float u0 = x32_add(sum4(acc[0]), sum4(acc[4])), u1 = x32_add(sum4(acc[1]), sum4(acc[5])), u2 = x32_add(sum4(acc[2]), sum4(acc[6])), u3 = x32_add(sum4(acc[3]), sum4(acc[7]));
      const float z0 = x16_add(u0, u2) * inv, z1 = x16_add(u1, u3) * inv;
      const unsigned p0 = pk(z0, dpp<0xb1>(z0)), p1 = pk(z1, dpp<0xb1>(z1));
      const v4u unit = {nb(p0), nb(dppu<0x4e>(p0)), nb(p1), nb(dppu<0x4e>(p1))};
      if (p4 == 0) st_sc1(rws, co, unit);
    }
    if (isT2) {
      LANE_CONSTS();
      bar();
      const unsigned tf = latest4(ctl + C_TF);
      v4u wf[2][4];
      LAS char* wp = L + L_T2W + w * 16384 + pp * 8192 + rd128;
#pragma unroll
      for (int kk = 0; kk < 2; ++kk)
#pragma unroll
        for (int t = 0; t < 4; ++t) wf[kk][t] = tr16(wp + kk * 4096 + (((2 * t) ^ xr128) * 16), 2048);
      const unsigned ho = opaque(WS_HP + (unsigned)((w * 8 + xcd) * 2560 + rr * 256 + pp * 128 + r8 * 16));
      LAS f32x4* const rp = opaque((LAS f32x4*)(L + L_RED2 + c8 * 4384 + (8 * pp + r8) * 272 + w * 16));
      LAS char* const rw = opaque(L + L_RED2 + wave * 4384 + g * 272 + n * 16);
      const unsigned cxo = opaque(WS_CX + (unsigned)(((4 * xcd + w) * 16 + (n & 7)) * 256 + pp * 128 + (n >> 3) * 64 + g * 16));
      lgkm0();
      v4u u[2];
      wait_until(tf + D_CX);
      poll_blank2s<8 * 256>(rws, cxo, u);
      {
        v4u k0, k1;
#pragma unroll
        for (int i = 0; i < 4; ++i) {
          k0[i] = (unsigned)__builtin_amdgcn_update_dpp((int)u[0][i], (int)u[1][i], 0x128, 0xf, 0xc, false);
          k1[i] = (unsigned)__builtin_amdgcn_update_dpp((int)u[1][i], (int)u[0][i], 0x128, 0xf, 0x3, false);
        }
        u[0] = k0; u[1] = k1;
      }
      f32x4 acc[4];
#pragma unroll
      for (int t = 0; t < 4; ++t) acc[t] = mfma(wf[0][t], u[0], f32x4{0.f, 0.f, 0.f, 0.f});
#pragma unroll
      for (int t = 0; t < 4; ++t) acc[t] = mfma(wf[1][t], u[1], acc[t]);
#pragma unroll
      for (int t = 0; t < 4; ++t) *(LAS f32x4*)(rw + t * 1088) = acc[t];
      bar();
      {
        f32x4 s = (rp[0] + rp[4]) + (rp[8] + rp[12]);
#pragma unroll
        for (int i = 0; i < 4; ++i) { s[i] += dpp<0xb1>(s[i]); s[i] += dpp<0x4e>(s[i]); s[i] += dpp<0x141>(s[i]); }
        if (c8 == 0) st_sc1(rws, ho, v4u{nbf(s[0]), nbf(s[1]), nbf(s[2]), nbf(s[3])});
      }
      if (wave == 0) {
        const int idx = xcd * 10 + rr, b4 = idx & 3, mr = idx >> 2;
        const v2u xv = __builtin_amdgcn_raw_buffer_load_b64(rx, (unsigned)(b4 * 1280 + mr * 64 + c8 * 8), 0, 0);
        v4u hu[1];
        const unsigned th = now32();
        wait_until(th + 40);
        poll_blank1s<0>(rws, WS_HP + (unsigned)((b4 * 8 + r8) * 2560 + mr * 128 + c8 * 16), hu);
        *(LAS v4u*)(L + L_HS + lane * 16) = hu[0];
        lgkm0();
        LAS f32x4* hp = (LAS f32x4*)(L + L_HS + c8 * 16);
        const f32x4 o = ((hp[0] + hp[8]) + (hp[16] + hp[24])) + ((hp[32] + hp[40]) + (hp[48] + hp[56]));
        const unsigned oa = pk(o[0], o[1]), ob = pk(o[2], o[3]);
        unsigned ha = pk(lo16(xv.x) + lo16(oa), hi16(xv.x) + hi16(oa)), hb = pk(lo16(xv.y) + lo16(ob), hi16(xv.y) + hi16(ob));
        unsigned op = (unsigned)((b4 * MD + mr * 32 + lane * 4) * 2);
        asm volatile("" : "+v"(ha), "+v"(hb), "+v"(op));
        v4u u2[4];
        wait_until(th + D_OQ);
        poll_blank4m<8 * 2560>(rws, WS_OQ + (unsigned)((b4 * 27 + r8) * 2560 + mr * 128 + c8 * 16), 0xffffffull, u2);
        f32x4 s = (__builtin_bit_cast(f32x4, u2[0]) + __builtin_bit_cast(f32x4, u2[1])) + (__builtin_bit_cast(f32x4, u2[2]) + __builtin_bit_cast(f32x4, u2[3]));
#pragma unroll
        for (int i = 0; i < 4; ++i) { s[i] += dpp<0x128>(s[i]); s[i] = x32_sum(x16_sum(s[i])); }
        const unsigned sa = pk(s[0], s[1]), sb = pk(s[2], s[3]);
        if (lane < 8) __builtin_amdgcn_raw_buffer_store_b64(v2u{pk(lo16(ha) + lo16(sa), hi16(ha) + hi16(sa)), pk(lo16(hb) + lo16(sb), hi16(hb) + hi16(sb))}, mk_rsrc(out, NB * MD * 2), op, 0, 0);
        if (lane == 0 && idx == 0) __builtin_amdgcn_raw_buffer_store_b32(tag, rws, WS_EPOCH, 0, AUX_SC1);
        asm volatile(".Lmla_end_a:");
      }
    }
  } else if (wave < 4) {
    const int w = wave;
    LANE_CONSTS();
    __builtin_amdgcn_s_setprio(2);
    if (w >= 2) GET_TAG(4);
    lds_wait_ge<4>(ctl + C_QP, 1u);
    lds_wait_ge<4>(ctl + C_CK + w, 1u);
    if (w == 3) {
      v4u ev[2];
      poll_tag2s<1024>(rws, WS_EV + (unsigned)(lane * 16), tag, ev);
      lds_st(ctl + C_EV, 1u);
    } else lds_wait_ge<4>(ctl + C_EV, 1u);
    LAS char* const mxw = opaque(L + L_MX + n * 32 + (w * 2 + (lane >> 5)) * 4);
    LAS char* const mxr = opaque(L + L_MX + n * 32);
    LAS char* const pw = opaque(L + L_P2 + w * 1024 + lane * 16);
    {
      v4u u[8];
      v32u c0, c1, c2, c3;
      LAS char* const qw = opaque(L + L_Q + n * 1040 + g * 256 + w * 16);
      LAS char* const qw2 = opaque(L + L_CK + (n & 7) * 1040 + g * 256 + w * 16);
      LAS char* const qr = opaque(L + L_Q + lane * 16);
      LAS char* const qr2 = opaque(L + L_CK + lane * 16);
      const int b3k = (n >> 3) & 1;
      LAS char* const pe = opaque(L + L_CK + (32 * w + n) * CK_STR + g * 256 + b3k * 16);
      LAS char* const po = opaque(L + L_CK + (32 * w + n) * CK_STR + g * 256 - b3k * 16);
      const unsigned qa = opaque(WS_QA + (unsigned)((chunk >> 5) * 32768 + w * 1024 + lane * 16));
      lds_wait_ge<1>(ctl + C_CK + w, 1u);
      c_rows0(pe, po, c0, c1);
      wait_ck_or(ctl + C_CK + w, tstart + QA_AT);
      issue_blank8<4096>(rws, qa, u);
      lds_wait_ge(ctl + C_CK + w, 2u);
      c_rows1(pe, po, c2, c3);
      finish_blank8<4096>(rws, qa, u);
#pragma unroll
      for (int i = 0; i < 4; ++i) *(LAS v4u*)(qw + i * 64) = u[i];
      if (n < 8) {
#pragma unroll
        for (int i = 0; i < 4; ++i) *(LAS v4u*)(qw + 16 * 1040 + i * 64) = u[4 + i];
      }
      if (last_of<4>(ctl + C_HN)) { const unsigned t = now32() + D_ATT; st_sc1(rws, WS_H1 + (unsigned)((r8 * 128 + chunk) * 128 + c8 * 16), v4u{t, tag, t, tag}); }
      bar();
      scores_softmax(c0, c1, c2, c3, u, qr, qr2, qw2, mxw, mxr, pw, lds_addr(ctl + C_QN));
    }
    __builtin_amdgcn_s_setprio(0);
    if (isT3) {
      LANE_CONSTS();
      lds_wait_ge<2>(ctl + C_OS, 4u);
      {
        const rsrc_t r1 = mk_rsrc(w1, MD * FD * 2);
        const unsigned vo = (unsigned)(160 * w + r8) * 3456u + (unsigned)(jr * 128) + sw128;
#pragma unroll 1
        for (int k = 0; k < 4; ++k) { const unsigned qq = (unsigned)((k + b3) & 3) * 5u; dma_run<5, 1024, 27648>(lbase + L_T3W + w * 20480 + qq * 1024, vo, r1, qq * 27648u); }
      }
      vm_wait<0>();
      lds_st(ctl + C_WF + w, 1u);
      const rsrc_t r2 = mk_rsrc(w2, FD * MD * 2);
      const unsigned vo = (unsigned)(64 * jr + 32 * (n >> 3) + 8 * g) * 1280u + (unsigned)(w * 128 + (n & 7) * 16);
      v4u wr[3][8];
#pragma unroll
      for (int k = 0; k < 2; ++k)
#pragma unroll
        for (int i = 0; i < 8; ++i) { wr[k][i] = __builtin_amdgcn_raw_buffer_load_b128(r2, vo + k * 512, i * 1280, AUX_NT); }
#pragma unroll
      for (int i = 0; i < 8; ++i) { wr[2][i] = __builtin_amdgcn_raw_buffer_load_b128(r2, w < 2 ? vo + 1024 : vo, i * 1280, AUX_NT); }
      v32u wt[3];
#pragma unroll
      for (int k = 0; k < 3; ++k)
#pragma unroll
        for (int e = 0; e < 8; ++e)
#pragma unroll
          for (int i = 0; i < 4; ++i) wt[k][4 * e + i] = __builtin_amdgcn_perm(wr[k][2 * i + 1][e >> 1], wr[k][2 * i][e >> 1], (e & 1) ? 0x07060302u : 0x05040100u);
      asm volatile("" : "+{v[64:95]}"(wt[0]), "+{v[96:127]}"(wt[1]), "+{v[128:159]}"(wt[2]));
      const unsigned oq = opaque(n < 8 && g < 2 ? WS_OQ + (unsigned)((b3 * 27 + jr) * 2560 + w * 256 + n * 32 + g * 16) : 0x7f000000u);
      LAS float* const rd = opaque((LAS float*)(L + L_RED3 + lane * 4));
      LAS u16* const fw = opaque((LAS u16*)(L + L_F + w * 128 + lane * 2));
      LAS v4u* const fr = opaque((LAS v4u*)(L + L_F + w * 128 + (n & 1) * 64 + g * 16));
      lds_wait_ge<4>(ctl + C_CS, 4u);
      float cs = (rd[256] + rd[320]) + (rd[384] + rd[448]);
      asm volatile("" : "+v"(cs));
      lds_wait_ge<1>(ctl + C_CNT, 4u);
      const f32x4 s1 = *(LAS f32x4*)(L + L_RED3 + 2048), s2 = *(LAS f32x4*)(L + L_RED3 + 2064);
      const float mean = sum4(s1) * (1.f / MD), var = fmaxf(sum4(s2) * (1.f / MD) - mean * mean, 0.f);
      float rs = rcp(rbf(__builtin_amdgcn_sqrtf(rbf(rbf(var) + 1e-5f)))), mc = rbf(mean) * cs;
      asm volatile("" : "+v"(rs), "+v"(mc));
      bar();
      v4u af;
      {
        const float a = rbf((((rd[0] + rd[64]) + (rd[128] + rd[192])) - mc) * rs);
        const float f = a * rcp(1.f + ex2(-1.4426950408889634f * a));
        *fw = (u16)pk(f, f);
        asm volatile("" ::: "memory");
        af = *fr;
      }
      mlp2_publish(af, wt[0], wt[1], wt[2], rws, oq, w);
      asm volatile(".Lmla_end_b:");
    }
  } else {
    const int w = wave - 4;
    LANE_CONSTS();
    {
      if (isT3 && lane < 40) st_sc1(rws, WS_OQ + (unsigned)((b3 * 27 + jr) * 2560 + (w * 40 + lane) * 16), v4u{BLANK, BLANK, BLANK, BLANK});
      const rsrc_t rc = mk_rsrc(ckv, NB * SK * DC * 2);
      const unsigned v0 = (unsigned)lane * 16u, v1 = (unsigned)(lane ^ 1) * 16u;
      const unsigned sb = (unsigned)chunk * 131072u + (unsigned)w * 32768u;
      wait_until(tstart + CKV_AT);
      const unsigned l0 = lbase + L_CK + 32 * w * CK_STR;
      dma_run<8, CK_STR, 1024, CKV_WIN>(l0, v0, rc, sb);
      dma_run<8, CK_STR, 1024, CKV_WIN>(l0 + 8 * CK_STR, v1, rc, sb + 8192u);
      dma_run<8, CK_STR, 1024, CKV_WIN>(l0 + 16 * CK_STR, v0, rc, sb + 16384u);
      static_assert(CKV_WIN <= 8, "");
      lds_st(ctl + C_CK + w, 1u);
      dma_run<8, CK_STR, 1024, CKV_WIN>(l0 + 24 * CK_STR, v1, rc, sb + 24576u);
      vm_wait<0>();
      lds_st(ctl + C_CK + w, 2u);
      lds_add1(ctl + C_CKA);
    }
    GET_TAG(0);
    v32u t0, t1, t2, t3;
    LAS char* const pr = opaque(L + L_P2 + lane * 16);
    LAS char* const mxr = opaque(L + L_MX + (lane & 31) * 32);
    const unsigned ol = opaque(WS_OP + (unsigned)(((chunk * 32 + (n & 7)) * 64 + 16 * w + (n >> 3) * 4 + g) * 16));
    const unsigned fo = opaque(FL_OFF(lane & 31, w, chunk));
    {
      LAS char* const c0 = opaque(L + L_CK + (4 * g + q4) * CK_STR + w * 256 + (((p4 >> 1) ^ (g >> 1)) * 16) + (p4 & 1) * 8);
      LAS char* const c1 = opaque(c0 + 48 * CK_STR);
      LAS char* const c2 = opaque(c0 + 96 * CK_STR);
      lds_wait_ge(ctl + C_CKA, 4u);
      c_cols(c0, c1, c2, t0, t1, t2, t3);
    }
    bar();
    {
      pv_publish(t0, t1, t2, t3, pr, mxr, rws, ol, fo, tag);
      if (isT3) {
        if (w == 0) {
          v4u hv[2];
          poll_tag2s<64 * 128>(rws, WS_H1 + (unsigned)((xcd * 128 + lane) * 128), tag, hv);
          const unsigned base = __builtin_amdgcn_readfirstlane(hv[0].x);
          int m[2];
          hint_max(hv, base, m);
          const auto r2 = __builtin_amdgcn_permlane32_swap((unsigned)max(m[0], m[1]), (unsigned)max(m[0], m[1]), false, false);
          ctl[C_TF] = base + (unsigned)max((int)r2[0], (int)r2[1]);
        }
      }
      lds_add1(ctl + C_OS);
      __builtin_amdgcn_s_setprio(0);
    }
    if (isT3) {
      LANE_CONSTS();
      const int nn = n < 10 ? n : 9, un = 40 * w + 8 * (nn >> 1) + 4 * (nn & 1) + g;
      const v2u xv = __builtin_amdgcn_raw_buffer_load_b64(rx, (unsigned)(b3 * 1280 + un * 8), 0, 0);
      lds_wait_ge<4>(ctl + C_WF + w, 1u);
      v4u wf[5][4];
      LAS char* wp = L + L_T3W + w * 20480 + rd128;
#pragma unroll
      for (int kk = 0; kk < 5; ++kk)
#pragma unroll
        for (int t = 0; t < 4; ++t) wf[kk][t] = tr16(wp + kk * 4096 + (((2 * t) ^ xr128) * 16), 2048);
      {
        const v4u ones = {0x3f803f80u, 0x3f803f80u, 0x3f803f80u, 0x3f803f80u};
        f32x4 cs[4];
#pragma unroll
        for (int t = 0; t < 4; ++t) cs[t] = mfma(wf[0][t], ones, f32x4{0.f, 0.f, 0.f, 0.f});
#pragma unroll
        for (int kk = 1; kk < 5; ++kk)
#pragma unroll
          for (int t = 0; t < 4; ++t) cs[t] = mfma(wf[kk][t], ones, cs[t]);
        if (n == 0) {
#pragma unroll
          for (int t = 0; t < 4; ++t) *(LAS f32x4*)(L + L_RED3 + 1024 + w * 256 + (t * 4 + g) * 16) = cs[t];
        }
        lds_add1(ctl + C_CS);
      }
      v32u wa, wb; v16u wc;
#pragma unroll
      for (int i = 0; i < 32; ++i) { wa[i] = wf[i >> 4][(i >> 2) & 3][i & 3]; wb[i] = wf[2 + (i >> 4)][(i >> 2) & 3][i & 3]; }
#pragma unroll
      for (int i = 0; i < 16; ++i) wc[i] = wf[4][i >> 2][i & 3];
      asm volatile("" : "+{v[64:95]}"(wa), "+{v[96:127]}"(wb), "+{v[128:143]}"(wc));
      f32x4 xf = {lo16(xv.x), hi16(xv.x), lo16(xv.y), hi16(xv.y)};
      float act = n < 10 ? 1.f : 0.f;
      LAS float* const stw = opaque((LAS float*)(L + L_RED3 + 2048 + (g & 1) * 16 + w * 4));
      LAS char* const dw = opaque(L + L_RED3 + w * 256 + g * 16);
      asm volatile("" : "+v"(xf), "+v"(act));
      v4u u[8];
      lds_wait_ge(ctl + C_OS, 4u);
      wait_until(__builtin_amdgcn_readfirstlane(ctl[C_TF]) + D_HP);
      poll_blank8<2560>(rws, WS_HP + (unsigned)(b3 * 8 * 2560 + un * 16), u);
#define F4(i) __builtin_bit_cast(f32x4, u[i])
      const f32x4 o = ((F4(0) + F4(1)) + (F4(2) + F4(3))) + ((F4(4) + F4(5)) + (F4(6) + F4(7)));
#undef F4
      const unsigned oa = pk(o[0], o[1]), ob = pk(o[2], o[3]);
      const unsigned h0 = pk(xf[0] + lo16(oa), xf[1] + hi16(oa)), h1 = pk(xf[2] + lo16(ob), xf[3] + hi16(ob));
      mlp1_partial(h0, h1, wa, wb, wc, act, stw, dw, lds_addr(ctl + C_CNT));
      asm volatile(".Lmla_end_c:");
    }
  }
}
}

extern "C" void kernel_launch(void* const* d_in, const int* in_sizes, int n_in, void* d_out, int out_size, void* d_ws, size_t ws_size,
                              hipStream_t stream) {
  using namespace mla;
  (void)in_sizes; (void)n_in; (void)out_size;
  const size_t wsb = ws_size >= (16u << 20) ? (ws_size - (8u << 20)) & ~(size_t)0xfffff : 0;
  (void)hipFuncSetAttribute((const void*)mla_block, hipFuncAttributeMaxDynamicSharedMemorySize, L_BYTES);
  mla_block<<<256, 512, L_BYTES, stream>>>((const u16*)d_in[2], (const u16*)d_in[0], (unsigned char*)d_ws + wsb, (const u16*)d_in[3], (const u16*)d_in[1],
                                           (const u16*)d_in[4], (const u16*)d_in[5], (const u16*)d_in[7], (const u16*)d_in[8], (u16*)d_out);
}
```
